```python
import torch

def _softmax(x, axis=-1):
    return torch.softmax(x, dim=axis)
def _gelu(x):
    return torch.nn.functional.gelu(x, approximate="tanh")
def _silu(x):
    return torch.nn.functional.silu(x)
def _sigmoid(x):
    return torch.sigmoid(x)
def _layernorm(x, axis=-1, eps=1e-5):
    m = x.mean(dim=axis, keepdim=True)
    v = x.var(dim=axis, keepdim=True, unbiased=False)
    return (x - m) / torch.sqrt(v + eps)
def _scatter_add(tgt, idx, src, axis=0):
    return tgt.clone().index_add_(axis, idx, src)
def _segment_sum(src, seg, n):
    out = torch.zeros((n,) + src.shape[1:], dtype=src.dtype)
    return out.index_add_(0, seg, src)
def _pad(x, pw):
    flat = tuple(v for pair in reversed(pw) for v in pair)
    return torch.nn.functional.pad(x, flat)

kernel_name = 'parallel_sum__compressed_kv_cla__dense_activation_gelu__prompt'

def setup_inputs():

    _w = (lambda a: (lambda t: t.to(torch.bfloat16) if t.is_floating_point() else t)(torch.as_tensor(a)))
    def _A_setup():
        B, H, Sq, Sk, D, M = 1, 32, 2048, 2048, 128, 1024
        Dc = 4 * D
        return {
            "x": _w((lambda *s: torch.randn(*s, dtype=torch.bfloat16) * 0.05)(B, Sq, M)),
            "c_kv": _w((lambda *s: torch.randn(*s, dtype=torch.bfloat16) * 0.4)(B, Sk, Dc)),
            "w_q": _w((lambda *s: torch.randn(*s, dtype=torch.bfloat16) * 3.2)(M, H, D) * (M ** -0.5)),
            "w_uk": _w((lambda *s: torch.randn(*s, dtype=torch.bfloat16) * 3.2)(Dc, H, D) * (Dc ** -0.5)),
            "w_uv": _w((lambda *s: torch.randn(*s, dtype=torch.bfloat16) * 0.4)(Dc, H, D) * (Dc ** -0.5)),
            "w_o": _w((lambda *s: torch.randn(*s, dtype=torch.bfloat16) * 0.4)(H, D, M) * ((H * D) ** -0.5)),
        }
    _A_in = _A_setup()

    def _B_setup():
        B, S, D, F = 1, 2048, 1024, 3584
        return {
            "x": _w((lambda *s: torch.randn(*s, dtype=torch.bfloat16) * 0.05)(B, S, D)),
            "w1": _w((lambda *s: torch.randn(*s, dtype=torch.bfloat16) * 0.4)(D, F) * (D ** -0.5)),
            "b1": _w((lambda *s: torch.randn(*s, dtype=torch.bfloat16) * 12.8)(F) * 0.01),
            "w2": _w((lambda *s: torch.randn(*s, dtype=torch.bfloat16) * 0.2)(F, D) * (F ** -0.5)),
            "b2": _w((lambda *s: torch.randn(*s, dtype=torch.bfloat16) * 1.6)(D) * 0.01),
        }
    _B_in = _B_setup()

    return {
        "A_x": _A_in["x"],
        "A_c_kv": _A_in["c_kv"],
        "A_w_q": _A_in["w_q"],
        "A_w_uk": _A_in["w_uk"],
        "A_w_uv": _A_in["w_uv"],
        "A_w_o": _A_in["w_o"],
        "B_x": _B_in["x"],
        "B_w1": _B_in["w1"],
        "B_b1": _B_in["b1"],
        "B_w2": _B_in["w2"],
        "B_b2": _B_in["b2"],
    }

def reference(A_x, A_c_kv, A_w_q, A_w_uk, A_w_uv, A_w_o, B_x, B_w1, B_b1, B_w2, B_b2):
    def _A_ref(x, c_kv, w_q, w_uk, w_uv, w_o):
        B, Sq, M = x.shape[0], x.shape[1], x.shape[2]
        Sk = c_kv.shape[1]
        H, D = w_q.shape[1], w_q.shape[2]
        q = torch.einsum("bqm,mhd->bhqd", x, w_q)
        k = torch.einsum("bkc,chd->bhkd", c_kv, w_uk)
        v = torch.einsum("bkc,chd->bhkd", c_kv, w_uv)
        scores = torch.einsum("bhqd,bhkd->bhqk", q, k) * (D ** -0.5)
        off = Sk - Sq
        qi = torch.arange(Sq)[:, None]
        kj = torch.arange(Sk)[None, :]
        cmask = kj <= (qi + off)
        scores = torch.where(cmask[None, None], scores, -1e9)
        p = _softmax(scores, axis=-1)
        ctx = torch.einsum("bhqk,bhkd->bhqd", p, v)
        return torch.einsum("bhqd,hdm->bqm", ctx, w_o)

    def _B_ref(x, w1, b1, w2, b2):
        h = torch.einsum("bsd,df->bsf", x, w1) + b1
        h = _gelu(h)
        return torch.einsum("bsf,fd->bsd", h, w2) + b2

    x = A_x
    xn = _layernorm(x, axis=-1)
    return x + _A_ref(xn, A_c_kv, A_w_q, A_w_uk, A_w_uv, A_w_o) + _B_ref(xn, B_w1, B_b1, B_w2, B_b2)
```

```cpp
#include <hip/hip_runtime.h>
#include <hip/hip_bf16.h>
typedef unsigned short u16;
__device__ inline float bf2f(u16 b){ return __uint_as_float(((unsigned)b)<<16); }
__device__ inline u16 f2bf(float f){ unsigned u=__float_as_uint(f); u += 0x7FFFu + ((u>>16)&1u); return (u16)(u>>16); }

__global__ void ln_k(const u16* x, u16* xn){
  int row=blockIdx.x; __shared__ float red[256];
  float v[4]; float s=0; for(int i=0;i<4;i++){ v[i]=bf2f(x[row*1024+threadIdx.x+256*i]); s+=v[i]; }
  red[threadIdx.x]=s; __syncthreads(); for(int o=128;o>0;o>>=1){ if(threadIdx.x<o) red[threadIdx.x]+=red[threadIdx.x+o]; __syncthreads(); }
  float m=red[0]/1024.f; __syncthreads();
  float q=0; for(int i=0;i<4;i++){ float d=v[i]-m; q+=d*d; }
  red[threadIdx.x]=q; __syncthreads(); for(int o=128;o>0;o>>=1){ if(threadIdx.x<o) red[threadIdx.x]+=red[threadIdx.x+o]; __syncthreads(); }
  float r=1.f/sqrtf(red[0]/1024.f+1e-5f);
  for(int i=0;i<4;i++) xn[row*1024+threadIdx.x+256*i]=f2bf((v[i]-m)*r);
}
__global__ void gemm_k(const u16* A,int lda,const u16* B,int ldb,u16* C,int ldc,float* Cf,int M,int N,int K,const u16* bias,int mode){
  __shared__ float As[16][65]; __shared__ float Bs[16][65];
  int tx=threadIdx.x&15, ty=threadIdx.x>>4; int m0=blockIdx.y*64,n0=blockIdx.x*64;
  float acc[4][4]={};
  for(int k0=0;k0<K;k0+=16){
    for(int i=threadIdx.x;i<1024;i+=256){ int r=i>>4,c=i&15; As[c][r]=bf2f(A[(size_t)(m0+r)*lda+k0+c]); }
    for(int i=threadIdx.x;i<1024;i+=256){ int r=i>>6,c=i&63; Bs[r][c]=bf2f(B[(size_t)(k0+r)*ldb+n0+c]); }
    __syncthreads();
    for(int k=0;k<16;k++){ float a[4],b[4]; for(int i=0;i<4;i++){a[i]=As[k][ty*4+i]; b[i]=Bs[k][tx*4+i];}
      for(int i=0;i<4;i++)for(int j=0;j<4;j++)acc[i][j]+=a[i]*b[j]; }
    __syncthreads();
  }
  for(int i=0;i<4;i++)for(int j=0;j<4;j++){ int m=m0+ty*4+i,n=n0+tx*4+j; float v=acc[i][j];
    if(mode==1){ v+=bf2f(bias[n]); float u=0.7978845608f*(v+0.044715f*v*v*v); v=0.5f*v*(1.f+tanhf(u)); C[(size_t)m*ldc+n]=f2bf(v);}
    else if(mode==0) C[(size_t)m*ldc+n]=f2bf(v);
    else if(mode==2) Cf[(size_t)m*ldc+n]=v;
    else if(mode==3) Cf[(size_t)m*ldc+n]+=v; }
}
__global__ void attn_k(const u16* Q,const u16* Kt,const u16* V,u16* O){
  int q=blockIdx.x,h=blockIdx.y; __shared__ float qs[128]; __shared__ float p[2048]; __shared__ float red[64];
  int t=threadIdx.x; qs[t]=bf2f(Q[(size_t)q*4096+h*128+t]); qs[t+64]=bf2f(Q[(size_t)q*4096+h*128+t+64]); __syncthreads();
  float mx=-1e30f;
  for(int k=t;k<=q;k+=64){ float s=0; for(int d=0;d<128;d++) s+=qs[d]*bf2f(Kt[(size_t)k*4096+h*128+d]); s*=0.08838834764f; p[k]=s; mx=fmaxf(mx,s);}
  red[t]=mx; __syncthreads(); for(int o=32;o>0;o>>=1){ if(t<o) red[t]=fmaxf(red[t],red[t+o]); __syncthreads(); } mx=red[0]; __syncthreads();
  float sum=0; for(int k=t;k<=q;k+=64){ float e=__expf(p[k]-mx); p[k]=e; sum+=e; }
  red[t]=sum; __syncthreads(); for(int o=32;o>0;o>>=1){ if(t<o) red[t]+=red[t+o]; __syncthreads(); } sum=red[0];
  float a0=0,a1=0; for(int k=0;k<=q;k++){ a0+=p[k]*bf2f(V[(size_t)k*4096+h*128+t]); a1+=p[k]*bf2f(V[(size_t)k*4096+h*128+t+64]); }
  O[(size_t)q*4096+h*128+t]=f2bf(a0/sum); O[(size_t)q*4096+h*128+t+64]=f2bf(a1/sum);
}
__global__ void fin_k(const u16* x,const float* acc,const u16* b2,u16* out){ int i=blockIdx.x*256+threadIdx.x; out[i]=f2bf(bf2f(x[i])+acc[i]+bf2f(b2[i&1023])); }

extern "C" void kernel_launch(void* const* d_in, const int* in_sizes, int n_in, void* d_out, int out_size, void* d_ws, size_t ws_size, hipStream_t stream) {
  const u16* x=(const u16*)d_in[0]; const u16* ckv=(const u16*)d_in[1]; const u16* wq=(const u16*)d_in[2]; const u16* wuk=(const u16*)d_in[3]; const u16* wuv=(const u16*)d_in[4]; const u16* wo=(const u16*)d_in[5];
  const u16* w1=(const u16*)d_in[7]; const u16* b1=(const u16*)d_in[8]; const u16* w2=(const u16*)d_in[9]; const u16* b2=(const u16*)d_in[10];
  char* ws=(char*)d_ws; u16* xn=(u16*)ws; u16* q=(u16*)(ws+(4<<20)); u16* k=(u16*)(ws+(24<<20)); u16* v=(u16*)(ws+(44<<20)); u16* ctx=(u16*)(ws+(64<<20)); u16* h=(u16*)(ws+(84<<20)); float* acc=(float*)(ws+(104<<20));
  ln_k<<<2048,256,0,stream>>>(x,xn);
  gemm_k<<<dim3(64,32),256,0,stream>>>(xn,1024,wq,4096,q,4096,nullptr,2048,4096,1024,nullptr,0);
  gemm_k<<<dim3(64,32),256,0,stream>>>(ckv,512,wuk,4096,k,4096,nullptr,2048,4096,512,nullptr,0);
  gemm_k<<<dim3(64,32),256,0,stream>>>(ckv,512,wuv,4096,v,4096,nullptr,2048,4096,512,nullptr,0);
  gemm_k<<<dim3(56,32),256,0,stream>>>(xn,1024,w1,3584,h,3584,nullptr,2048,3584,1024,b1,1);
  attn_k<<<dim3(2048,32),64,0,stream>>>(q,k,v,ctx);
  gemm_k<<<dim3(16,32),256,0,stream>>>(ctx,4096,wo,1024,nullptr,1024,acc,2048,1024,4096,nullptr,2);
  gemm_k<<<dim3(16,32),256,0,stream>>>(h,3584,w2,1024,nullptr,1024,acc,2048,1024,3584,nullptr,3);
  fin_k<<<2048*1024/256,256,0,stream>>>(x,acc,b2,(u16*)d_out);
}
```

```cpp
#include <hip/hip_runtime.h>
#include <stdint.h>
#include <utility>
#define ROT 0
#define ROT0 0
#define ROT1 0
#define CODEPF 21
#define CODEPF2 0
#define TLBPF 0
#define PF0 0
#define LNW "36"
#define PRW "28"
#define ABL 0

typedef unsigned short bf16_t;
typedef short bf16x8 __attribute__((ext_vector_type(8)));
typedef short s16x4 __attribute__((ext_vector_type(4)));
typedef float f32x2 __attribute__((ext_vector_type(2)));
typedef float f32x4 __attribute__((ext_vector_type(4)));
typedef unsigned u32x4 __attribute__((ext_vector_type(4)));
typedef unsigned u32x2 __attribute__((ext_vector_type(2)));
typedef __bf16 bf16x2_t __attribute__((ext_vector_type(2)));
#define LAS __attribute__((address_space(3)))
#define DI __device__ __forceinline__
#define BAR __builtin_amdgcn_s_barrier()

constexpr int SEQ = 2048, DM = 1024, NH = 32, HD = 128, DC = 512, FF = 3584;
constexpr size_t MiB = 1u << 20;
constexpr size_t WS_XN = 0;
constexpr size_t WS_QF = 4 * MiB;
constexpr size_t WS_KF = 20 * MiB;
constexpr size_t WS_VF = 36 * MiB;
constexpr size_t WS_AF = 52 * MiB;
constexpr size_t WS_PART = 84 * MiB;
constexpr size_t WS_FLAG = 120 * MiB;

DI unsigned cvtpk(float lo, float hi) { f32x2 v = {lo, hi}; bf16x2_t b = __builtin_convertvector(v, bf16x2_t); return __builtin_bit_cast(unsigned, b); }
DI float bf_lo(unsigned w) { return __uint_as_float(w << 16); }
DI float bf_hi(unsigned w) { return __uint_as_float(w & 0xffff0000u); }

DI int opaque_lane() { int l; asm volatile("v_mbcnt_lo_u32_b32 %0, -1, 0\n\tv_mbcnt_hi_u32_b32 %0, -1, %0" : "=v"(l)); return l; }
typedef __amdgpu_buffer_rsrc_t rsrc_t;
DI rsrc_t mkrs(const void* base, unsigned bytes) { return __builtin_amdgcn_make_buffer_rsrc((void*)base, (short)0, (int)bytes, 0x00020000); }
DI void st16(rsrc_t rs, unsigned off, u32x4 v) { __builtin_amdgcn_raw_buffer_store_b128(v, rs, off, 0, 16); }
constexpr int W_E = 0, W_START = 64, W_XN = 320, W_KV = 448, W_Q = 1472, W_H = 2496, W_C = 3008, W_X = 5056, W_TMO = 9152;
DI unsigned* flagp(unsigned char* ws, int i) { return (unsigned*)(ws + WS_FLAG) + i; }
DI void flag_set(unsigned char* ws, int i, unsigned tag) { __hip_atomic_store(flagp(ws, i), tag, __ATOMIC_RELAXED, __HIP_MEMORY_SCOPE_AGENT); }
DI void flag_wait(unsigned char* ws, int idx, unsigned tag) {
    unsigned* f = flagp(ws, idx < 0 ? W_E : idx); const unsigned long long t0 = __builtin_amdgcn_s_memrealtime();
    for (;;) {
        const unsigned v = __hip_atomic_load(f, __ATOMIC_RELAXED, __HIP_MEMORY_SCOPE_AGENT);
        if (__all(idx < 0 || v == tag)) break;
        __builtin_amdgcn_s_sleep(1);
        if (__builtin_amdgcn_s_memrealtime() - t0 > 5000000ull) { __hip_atomic_store(flagp(ws, W_TMO), 1u, __ATOMIC_RELAXED, __HIP_MEMORY_SCOPE_AGENT); break; }
    }
}
DI void poll_issue(LAS unsigned char* scr, unsigned char* ws, int idx) {
    const unsigned* f = flagp(ws, idx < 0 ? W_E : idx);
    asm volatile("s_mov_b32 m0, %1\n\ts_nop 0\n\tglobal_load_lds_dword %0, off sc1" :: "v"(f), "s"((unsigned)(size_t)scr) : "memory", "m0");
}
DI void poll_finish(LAS unsigned char* scr, unsigned char* ws, int idx, unsigned tag) {
    const unsigned v = *(volatile LAS unsigned*)(scr + (threadIdx.x & 63) * 4);
    if (!__all(idx < 0 || v == tag)) flag_wait(ws, idx, tag);
}
DI void poll_finish_inv(LAS unsigned char* scr, unsigned char* ws, int idx, unsigned tag) {
    poll_finish(scr, ws, idx, tag);
    asm volatile("buffer_inv sc1" ::: "memory");
}
DI void acquire_all() { __builtin_amdgcn_fence(__ATOMIC_ACQUIRE, "agent"); asm volatile("s_waitcnt vmcnt(0)" ::: "memory"); }
#define DRAIN_BAR() do { asm volatile("s_waitcnt vmcnt(0) lgkmcnt(0)" ::: "memory"); __builtin_amdgcn_s_barrier(); asm volatile("" ::: "memory"); } while (0)

DI void ln_load(u32x4 (&v)[8], const bf16_t* __restrict__ x, int mb, int wid, int lane) {
    const bf16_t* src = x + (size_t)(mb * 16 + (lane & 15)) * DM + wid * 256 + (lane >> 4) * 8;
    asm volatile("global_load_dwordx4 %0, %8, off\n\tglobal_load_dwordx4 %1, %8, off offset:64\n\tglobal_load_dwordx4 %2, %8, off offset:128\n\tglobal_load_dwordx4 %3, %8, off offset:192\n\t"
                 "global_load_dwordx4 %4, %8, off offset:256\n\tglobal_load_dwordx4 %5, %8, off offset:320\n\tglobal_load_dwordx4 %6, %8, off offset:384\n\tglobal_load_dwordx4 %7, %8, off offset:448"
                 : "=&v"(v[0]), "=&v"(v[1]), "=&v"(v[2]), "=&v"(v[3]), "=&v"(v[4]), "=&v"(v[5]), "=&v"(v[6]), "=&v"(v[7]) : "v"(src) : "memory");
}
DI void ln_block(u32x4 (&v)[8], rsrc_t rs, LAS float* red, int mb, int wid, int lane) {
    const int fr = lane & 15, fq = lane >> 4;
    asm volatile("s_waitcnt vmcnt(" LNW ")" : "+v"(v[0]), "+v"(v[1]), "+v"(v[2]), "+v"(v[3]), "+v"(v[4]), "+v"(v[5]), "+v"(v[6]), "+v"(v[7]) :: "memory");
    float s = 0.f;
#pragma unroll
    for (int ks = 0; ks < 8; ++ks)
#pragma unroll
        for (int j = 0; j < 4; ++j) s += bf_lo(v[ks][j]) + bf_hi(v[ks][j]);
    s += __shfl_xor(s, 16); s += __shfl_xor(s, 32);
    if (fq == 0) red[wid * 16 + fr] = s;
    asm volatile("s_waitcnt lgkmcnt(0)" ::: "memory"); BAR; asm volatile("" ::: "memory");
    const float mean = (red[fr] + red[16 + fr] + red[32 + fr] + red[48 + fr]) * (1.0f / DM);
    float q = 0.f;
#pragma unroll
    for (int ks = 0; ks < 8; ++ks)
#pragma unroll
        for (int j = 0; j < 4; ++j) { const float a = bf_lo(v[ks][j]) - mean, b = bf_hi(v[ks][j]) - mean; q += a * a + b * b; }
    q += __shfl_xor(q, 16); q += __shfl_xor(q, 32);
    if (fq == 0) red[64 + wid * 16 + fr] = q;
    asm volatile("s_waitcnt lgkmcnt(0)" ::: "memory"); BAR; asm volatile("" ::: "memory");
    const float rstd = 1.0f / sqrtf((red[64 + fr] + red[80 + fr] + red[96 + fr] + red[112 + fr]) * (1.0f / DM) + 1e-5f);
#pragma unroll
    for (int ks = 0; ks < 8; ++ks) { u32x4 o;
#pragma unroll
        for (int j = 0; j < 4; ++j) o[j] = cvtpk((bf_lo(v[ks][j]) - mean) * rstd, (bf_hi(v[ks][j]) - mean) * rstd);
        st16(rs, (unsigned)((mb * 32 + wid * 8 + ks) * 1024 + lane * 16), o); }
}

constexpr int SLOT = 32768, NSLOT = 4;
constexpr int G4_LDS = SLOT * NSLOT;
enum { U_K = 0, U_V = 1, U_Q = 2, U_H = 3, U_ATT = 4 };
struct Unit { const char* A; const char* B; unsigned kstepA, kstepB, kjump; int nks, type, pm, pn; };
struct K1Params { const bf16_t *x, *ckv, *wq, *wuk, *wuv, *wo, *w1, *b1, *w2, *b2; bf16_t* out; unsigned char* ws; };
typedef K1Params K3Params;

DI void k1_unit(Unit& u, const K1Params& p, int type, int pm, int pn) {
    u.type = type; u.pm = pm; u.pn = pn; u.kjump = 0;
    if (type <= U_V) { u.A = (const char*)p.ckv + (size_t)pm * 256 * DC * 2; u.kstepA = 64; u.nks = DC / 32;
        u.B = (const char*)(type == U_K ? p.wuk : p.wuv) + (size_t)pn * 512; u.kstepB = 32 * 4096 * 2; }
    else if (type == U_ATT) {
        u.A = (const char*)p.ws + WS_KF + (size_t)pm * 128 * 4096; u.B = u.A + 16384; u.kstepA = u.kstepB = pn ? 32768u : 0u; u.kjump = (unsigned)(WS_VF - WS_KF) - 2u * u.kstepA; u.nks = 4; }
    else { u.A = (const char*)p.ws + WS_XN + (size_t)pm * 16 * 32 * 1024; u.kstepA = 1024; u.nks = DM / 32;
        if (type == U_Q) { u.B = (const char*)p.wq + (size_t)pn * 512; u.kstepB = 32 * 4096 * 2; }
        else { u.B = (const char*)p.w1 + (size_t)pn * 512; u.kstepB = 32 * FF * 2; } }
}
DI void k1_voff(int type, int wid, int lane, unsigned (&voff)[8]) {
    if (type == U_ATT) {
#pragma unroll
        for (int i = 0; i < 8; ++i) voff[i] = (unsigned)(wid * 4096 + (i & 3) * 1024 + lane * 16);
        return; }
    unsigned a0, st;
    if (type <= U_V) { const int i = lane >> 2, ch = (lane & 3) ^ ((i >> 3) << 1); a0 = (unsigned)((wid * 64 + i) * DC + ch * 8) * 2u; st = 16 * DC * 2; }
    else { a0 = (unsigned)(wid * 4 * 32 * 1024 + lane * 16); st = 32 * 1024; }
#pragma unroll
    for (int i = 0; i < 4; ++i) voff[i] = a0 + i * st;
    const int r = lane >> 3, c8 = (lane & 7) ^ ((((r >> 1) & 1) << 2) | (wid & 1));
    const int ldw = type == U_H ? FF : 4096;
#pragma unroll
    for (int i = 0; i < 4; ++i) voff[4 + i] = (unsigned)((wid * 8 + r) * ldw + c8 * 8) * 2u + i * 128;
}
DI unsigned k1_aoff(int type, int wr, int lane) {
    const int fr = lane & 15, fq = lane >> 4;
    return (unsigned)(wr * 8192 + (type <= U_V ? fr * 64 + ((fq ^ ((fr >> 3) << 1)) * 16) : lane * 16));
}
#define GLDS(gptr, ldsoff) __builtin_amdgcn_global_load_lds((const unsigned*)(gptr), (LAS unsigned*)(lds + (ldsoff)), 16, 0, 0)
#define TRD(dst, addr, off) asm volatile("ds_read_b64_tr_b16 %0, %1 offset:%2" : "=v"(dst) : "v"(addr), "i"(off) : "memory")
#define RD128(dst, addr, off) asm volatile("ds_read_b128 %0, %1 offset:%2" : "=v"(dst) : "v"(addr), "i"(off) : "memory")
#define WAIT_V(n) asm volatile("s_waitcnt vmcnt(" #n ")" ::: "memory")
#define WAIT_L(n) asm volatile("s_waitcnt lgkmcnt(" #n ")" ::: "memory")
#define SCHED __builtin_amdgcn_sched_barrier(0)

#define ACLOB "a0","a1","a2","a3","a4","a5","a6","a7","a8","a9","a10","a11","a12","a13","a14","a15","a16","a17","a18","a19","a20","a21","a22","a23","a24","a25","a26","a27","a28","a29","a30","a31","a32","a33","a34","a35","a36","a37","a38","a39","a40","a41","a42","a43","a44","a45","a46","a47","a48","a49","a50","a51","a52","a53","a54","a55","a56","a57","a58","a59","a60","a61","a62","a63","a64","a65","a66","a67","a68","a69","a70","a71","a72","a73","a74","a75","a76","a77","a78","a79","a80","a81","a82","a83","a84","a85","a86","a87","a88","a89","a90","a91","a92","a93","a94","a95","a96","a97","a98","a99","a100","a101","a102","a103","a104","a105","a106","a107","a108","a109","a110","a111","a112","a113","a114","a115","a116","a117","a118","a119","a120","a121","a122","a123","a124","a125","a126","a127","a128","a129","a130","a131","a132","a133","a134","a135","a136","a137","a138","a139","a140","a141","a142","a143","a144","a145","a146","a147","a148","a149","a150","a151","a152","a153","a154","a155","a156","a157","a158","a159","a160","a161","a162","a163","a164","a165","a166","a167","a168","a169","a170","a171","a172","a173","a174","a175","a176","a177","a178","a179","a180","a181","a182","a183","a184","a185","a186","a187","a188","a189","a190","a191","a192","a193","a194","a195","a196","a197","a198","a199","a200","a201","a202","a203","a204","a205","a206","a207","a208","a209","a210","a211","a212","a213","a214","a215","a216","a217","a218","a219","a220","a221","a222","a223","a224","a225","a226","a227","a228","a229","a230","a231","a232","a233","a234","a235","a236","a237","a238","a239","a240","a241","a242","a243","a244","a245","a246","a247","a248","a249","a250","a251","a252","a253","a254","a255"
struct Frags { bf16x8 a[8]; s16x4 bl[8], bh[8]; };

template <int I> DI void read1(Frags& f, unsigned aad, const unsigned (&bad)[4]) {
    RD128(f.a[I], aad, I * 1024);
    constexpr int cg = I >> 1, n = I & 1;
    TRD(f.bl[I], bad[(cg & 1) * 2 + n], (cg >> 1) * 1024); TRD(f.bh[I], bad[(cg & 1) * 2 + n], (cg >> 1) * 1024 + 512);
}
#define BF(n) __builtin_shufflevector(f.bl[n], f.bh[n], 0, 1, 2, 3, 4, 5, 6, 7)
#define MF1(i, c) "v_mfma_f32_16x16x32_bf16 a[%c[r]+" #i "*4:%c[r]+" #i "*4+3], %[b" #i "], %[a], " c "\n\t"
#define CC(i) "a[%c[r]+" #i "*4:%c[r]+" #i "*4+3]"
#define C0(i) "0"
#define ROW_ASM(C, PFL) \
        MF1(0, C(0)) "ds_read_b128 %[na], %[aad] offset:%c[ao]\n\t" \
        MF1(1, C(1)) "ds_read_b64_tr_b16 %[nl], %[bad] offset:%c[bo]\n\t" \
        MF1(2, C(2)) "ds_read_b64_tr_b16 %[nh], %[bad] offset:%c[bo]+512\n\t" \
        MF1(3, C(3)) "s_add_u32 m0, %[ldsb], %c[mo]\n\t" \
        MF1(4, C(4)) "global_load_lds_dwordx4 %[vo], %[sb]\n\t" \
        MF1(5, C(5)) MF1(6, C(6)) PFL MF1(7, C(7))
#define PF_ON "s_mov_b32 m0, %[pfm]\n\ts_nop 0\n\tglobal_load_lds_dwordx4 %[pfv], %[pfs]\n\t"
#define ROW_OPS(I) : [na] "=&v"(nx.a[I]), [nl] "=&v"(nx.bl[I]), [nh] "=&v"(nx.bh[I]) \
        : [b0] "v"(BF(0)), [b1] "v"(BF(1)), [b2] "v"(BF(2)), [b3] "v"(BF(3)), [b4] "v"(BF(4)), [b5] "v"(BF(5)), [b6] "v"(BF(6)), [b7] "v"(BF(7)), [a] "v"(f.a[I]), \
          [r] "i"(32 * I), [aad] "v"(aad), [ao] "i"(SO + I * 1024), [bad] "v"(bad[((I >> 1) & 1) * 2 + (I & 1)]), [bo] "i"(SO + (I >> 2) * 1024), \
          [ldsb] "s"(ldsb), [mo] "i"(I < 4 ? I * 1024 : 16384 + (I - 4) * 1024), [vo] "v"(voff[I]), [sb] "s"(I < 4 ? sa : sb), [pfv] "v"(pf.v), [pfs] "s"(pf.s), [pfm] "s"(pf.m) \
        : "memory", "scc", "m0", ACLOB
struct PfS { unsigned v, m; const char* s; };
template <bool FIRST, int SO, int I> DI void g4_row(PfS& pf, const Frags& f, Frags& nx, unsigned aad, const unsigned (&bad)[4], unsigned ldsb, const unsigned (&voff)[8], const char* sa, const char* sb) {
    if constexpr (I == 7) { if constexpr (FIRST) asm volatile(ROW_ASM(C0, PF_ON) ROW_OPS(I)); else asm volatile(ROW_ASM(CC, PF_ON) ROW_OPS(I)); }
    else { if constexpr (FIRST) asm volatile(ROW_ASM(C0, "") ROW_OPS(I)); else asm volatile(ROW_ASM(CC, "") ROW_OPS(I)); }
}
template <int I> DI void stage1(unsigned ldsb, const unsigned (&voff)[8], const char* sa, const char* sb) {
    asm volatile("s_add_u32 m0, %[ldsb], %c[mo]\n\ts_nop 4\n\tglobal_load_lds_dwordx4 %[vo], %[sb]"
        :: [ldsb] "s"(ldsb), [mo] "i"(I < 4 ? I * 1024 : 16384 + (I - 4) * 1024), [vo] "v"(voff[I]), [sb] "s"(I < 4 ? sa : sb) : "memory", "scc", "m0");
}
template <int M, int N> DI f32x4 acc_rd() { f32x4 v; constexpr int r = 4 * (8 * M + N);
    asm volatile("v_accvgpr_read_b32 %0, a%c4\n\tv_accvgpr_read_b32 %1, a%c5\n\tv_accvgpr_read_b32 %2, a%c6\n\tv_accvgpr_read_b32 %3, a%c7"
                 : "=v"(v[0]), "=v"(v[1]), "=v"(v[2]), "=v"(v[3]) : "i"(r), "i"(r + 1), "i"(r + 2), "i"(r + 3) : ACLOB);
    return v; }
#define PIN_FRAGS(f) do { asm volatile("s_waitcnt lgkmcnt(0)" : "+v"(f.a[0]), "+v"(f.a[1]), "+v"(f.a[2]), "+v"(f.a[3]), "+v"(f.a[4]), "+v"(f.a[5]), "+v"(f.a[6]), "+v"(f.a[7]), \
        "+v"(f.bl[0]), "+v"(f.bl[1]), "+v"(f.bl[2]), "+v"(f.bl[3]), "+v"(f.bl[4]), "+v"(f.bl[5]), "+v"(f.bl[6]), "+v"(f.bl[7]) :: "memory"); \
    asm volatile("" : "+v"(f.bh[0]), "+v"(f.bh[1]), "+v"(f.bh[2]), "+v"(f.bh[3]), "+v"(f.bh[4]), "+v"(f.bh[5]), "+v"(f.bh[6]), "+v"(f.bh[7]) :: "memory"); } while (0)

template <bool FIRST, int SL, int... Is> DI void g4_step_(PfS& pf, Frags& cur, Frags& nxt, const char* sa, const char* sb, const unsigned (&voff)[8],
                                   unsigned ldsb, unsigned aad, const unsigned (&bad)[4], std::integer_sequence<int, Is...>) {
    PIN_FRAGS(cur);
    WAIT_V(19); BAR;
    (g4_row<FIRST, ((SL + 1) & 1) * SLOT, Is>(pf, cur, nxt, aad, bad, ldsb, voff, sa, sb), ...);
}
struct G4Addr { unsigned aX, aY, a0, bX[4], bY[4], ldsb; };

DI const char* k1_pfbase(const K1Params& p, int gi, int wid) {
    const size_t b = blockIdx.x; const char* r;
    if (gi < 2) r = (const char*)p.wq + b * 32768 + gi * 16384;
    else if (gi < 4) r = (const char*)p.w1 + (b < 255 ? b * 28672 : (size_t)FF * DM * 2 - 32768) + (gi - 2) * 16384;
    else r = (const char*)p.w1 + (b < 255 ? b * 28672 : (size_t)FF * DM * 2 - 32768) + 16384;
    return r + wid * 1024;
}
DI void g4_kloop(LAS unsigned char* lds, const K1Params& p, PfS& pf, int& pfg, Frags& f0, Frags& f1, const Unit& cur, const Unit& nxt, unsigned (&voff)[8], G4Addr& ad,
                 const unsigned aoff_nxt, const int wid, const int lane, const int pend_flag, const unsigned tag, const int poll_idx, const int poll_j, const char* codep) {
    const int nks = cur.nks;
    const char* sa = cur.A + (size_t)4 * cur.kstepA; const char* sb = cur.B + (size_t)4 * cur.kstepB;
    unsigned kA = cur.kstepA, kB = cur.kstepB, kJ = 0;
#define G4_SWITCH(j) if ((j) + 4 == nks) { sa = nxt.A; sb = nxt.B; kA = nxt.kstepA; kB = nxt.kstepB; kJ = nxt.kjump; k1_voff(nxt.type, wid, lane, voff); ad.a0 = aoff_nxt; }
#define G4_GROUP(FIRST) do { \
        pf.s = k1_pfbase(p, pfg++, wid); \
        g4_step_<FIRST, 0>(pf, f0, f1, sa, sb, voff, ad.ldsb, ad.aX, ad.bX, std::make_integer_sequence<int, 8>{}); sa += kA; sb += kB; pf.s += 4096; \
        g4_step_<false, 1>(pf, f1, f0, sa, sb, voff, ad.ldsb + SLOT, ad.aY, ad.bY, std::make_integer_sequence<int, 8>{}); sa += kA + kJ; sb += kB + kJ; pf.s += 4096; \
        g4_step_<false, 2>(pf, f0, f1, sa, sb, voff, ad.ldsb + 2 * SLOT, ad.aY, ad.bY, std::make_integer_sequence<int, 8>{}); sa += kA; sb += kB; pf.s += 4096; \
        g4_step_<false, 3>(pf, f1, f0, sa, sb, voff, ad.ldsb + 3 * SLOT, ad.a0, ad.bX, std::make_integer_sequence<int, 8>{}); sa += kA; sb += kB; } while (0)
    G4_SWITCH(0);
    G4_GROUP(true);
    if (pend_flag >= 0 && wid == 0 && lane == 0) flag_set(p.ws, pend_flag, tag);
    for (int j = 4; j < nks; j += 4) {
        if (poll_j >= 0 && wid == 0) {
            if (j == poll_j) poll_issue(lds + 132096, p.ws, poll_idx); else if (j == poll_j + 4) poll_finish_inv(lds + 132096, p.ws, poll_idx, tag); }
        if (CODEPF2 && j == 24) asm volatile("s_mov_b32 m0, %2\n\ts_nop 4\n\tglobal_load_lds_dwordx4 %0, %1" :: "v"(pf.v), "s"(codep), "s"(pf.m) : "memory", "m0");
        G4_SWITCH(j); G4_GROUP(false); }
#undef G4_SWITCH
#undef G4_GROUP
    ad.aX = ad.a0; ad.aY = ad.a0 + 2 * SLOT;
    asm volatile("s_nop 7\n\ts_nop 7" ::: "memory");
}

DI u32x4 pack8(const f32x4& lo, const f32x4& hi) { u32x4 w; w[0] = cvtpk(lo[0], lo[1]); w[1] = cvtpk(lo[2], lo[3]); w[2] = cvtpk(hi[0], hi[1]); w[3] = cvtpk(hi[2], hi[3]); return w; }
DI u32x4 pack8s(const f32x4& a, const f32x4& b, bool odd) {
    const unsigned p0 = cvtpk(a[0], a[1]), p1 = cvtpk(a[2], a[3]), p2 = cvtpk(b[0], b[1]), p3 = cvtpk(b[2], b[3]);
    u32x4 w; w[0] = odd ? p2 : p0; w[1] = odd ? p3 : p1; w[2] = odd ? p0 : p2; w[3] = odd ? p1 : p3; return w; }
DI f32x2 gelu2(f32x2 v) {
    const float c1 = -2.0f * 0.7978845608028654f * 1.4426950408889634f, c2 = c1 * 0.044715f;
    const f32x2 a = v * (v * v * c2 + c1);
    f32x2 d; d.x = __builtin_amdgcn_exp2f(a.x); d.y = __builtin_amdgcn_exp2f(a.y); d = d + 1.0f;
    f32x2 r; r.x = __builtin_amdgcn_rcpf(d.x); r.y = __builtin_amdgcn_rcpf(d.y);
    return v * r;
}
template <int I> DI void epi_h1(rsrc_t rs, unsigned off, const bf16_t* b1p, bool odd, f32x4& ba, f32x4& bb) {
    constexpr int cg = I >> 3, m = I & 7;
    if constexpr (m == 0) { const u32x4 bw = *(const u32x4*)(b1p + cg * 32);
        const f32x4 lo = {bf_lo(bw[0]), bf_hi(bw[0]), bf_lo(bw[1]), bf_hi(bw[1])}, hi = {bf_lo(bw[2]), bf_hi(bw[2]), bf_lo(bw[3]), bf_hi(bw[3])};
        ba = odd ? hi : lo; bb = odd ? lo : hi; }
    const f32x4 a = acc_rd<m, 2 * cg>() + ba, b = acc_rd<m, 2 * cg + 1>() + bb;
    const f32x2 a0 = gelu2((f32x2){a[0], a[1]}), a1 = gelu2((f32x2){a[2], a[3]}), b0 = gelu2((f32x2){b[0], b[1]}), b1 = gelu2((f32x2){b[2], b[3]});
    st16(rs, off + (unsigned)(m * 240 * 1024 + cg * 1024), pack8s((f32x4){a0.x, a0.y, a1.x, a1.y}, (f32x4){b0.x, b0.y, b1.x, b1.y}, odd));
}
template <int I> DI void epi_v1(rsrc_t rs, unsigned off, bool odd) {
    constexpr int cg = I >> 3, m = I & 7;
    st16(rs, off + (unsigned)(m * 4096 + cg * 1024), pack8s(acc_rd<m, 2 * cg>(), acc_rd<m, 2 * cg + 1>(), odd));
}
template <int I> DI void epi_qk1(rsrc_t rs, unsigned off, bool odd, float sc) {
    constexpr int cg = I >> 3, m = I & 7;
    st16(rs, off + (unsigned)(m * 4096 + cg * 1024), pack8s(acc_rd<m, 2 * cg>() * sc, acc_rd<m, 2 * cg + 1>() * sc, odd));
}
template <int... Is> DI void k1_epi_(const Unit& u, const K1Params& p, int wr, int wc, int lane, std::integer_sequence<int, Is...>) {
    lane = opaque_lane();
    const int fr = lane & 15, fq = lane >> 4; const bool odd = fq & 1;
    if (u.type == U_H) {
        const int col = u.pn * 256 + wc * 128;
        f32x4 ba, bb;
        (epi_h1<Is>(mkrs(p.ws + WS_AF, 30u << 20), (unsigned)(lane * 16 + ((u.pm * 16 + wr * 8) * 240 + 128 + (col >> 5)) * 1024), p.b1 + col + fq * 8, odd, ba, bb), ...);
    } else if (u.type == U_V) {
        const int h = 2 * u.pn + wc;
        (epi_v1<Is>(mkrs(p.ws + WS_VF, 16u << 20), (unsigned)((h * 128 + u.pm * 16 + wr * 8) * 4096 + fr * 64 + ((fq ^ ((fr >> 2) & 1)) * 16)), odd), ...);
    } else {
        const float sc = u.type == U_Q ? 0.08838834764831845f * 1.4426950408889634f : 1.0f;
        const int h = 2 * u.pn + wc;
        (epi_qk1<Is>(mkrs(p.ws + (u.type == U_Q ? WS_QF : WS_KF), 16u << 20), (unsigned)(lane * 16 + (h * 128 + u.pm * 16 + wr * 8) * 4096), odd, sc), ...);
    }
}
DI void k1_epi_T(const Unit& u, const K1Params& p, int wr, int wc, int lane) { k1_epi_(u, p, wr, wc, lane, std::make_integer_sequence<int, 32>{}); }
DI int unit_flag(const Unit& u) { return u.type <= U_V ? W_KV + u.pn * 64 + u.type * 8 + u.pm : (u.type == U_Q ? W_Q + u.pn * 64 + u.pm : W_H + u.pm * 64 + u.pn); }

#define STAMP(i) do {} while (0)
template <int... Is> DI void stage_all(const Unit& u, int j, const unsigned (&voff)[8], unsigned ldsb, std::integer_sequence<int, Is...>) {
    (stage1<Is>(ldsb, voff, u.A + (size_t)j * u.kstepA, u.B + (size_t)j * u.kstepB), ...);
}
template <int... Is> DI void read_all(Frags& f, unsigned aad, const unsigned (&bad)[4], std::integer_sequence<int, Is...>) { (read1<Is>(f, aad, bad), ...); }
DI unsigned phase1(LAS unsigned char* lds, const K1Params& p, const int wid, const int lane, const int att_h, const int att_t1, const char* codep) {
    poll_issue(lds + 133120 + wid * 256, p.ws, -1);
    const int wr = wid >> 1, wc = wid & 1;
    const int xcd = blockIdx.x & 7, s = blockIdx.x >> 3;
    STAMP(0);
    Unit u0, u1;
    k1_unit(u0, p, (s >> 4) ? U_K : U_V, s & 7, 2 * xcd + ((s >> 3) & 1));
    const bool has2 = s < 30;
    if (s < 16) k1_unit(u1, p, U_Q, s & 7, 2 * xcd + (s >> 3));
    else if (s < 30) { const int L = 14 * xcd + (s - 16); k1_unit(u1, p, U_H, L & 7, L >> 3); }
    else u1 = u0;
    unsigned boff[4]; { const int g = lane >> 4, q = (lane >> 2) & 3, pp = lane & 3;
        const unsigned b0 = 16384u + (unsigned)((g * 4 + wc * 2) * 1024 + q * 128 + ((pp ^ (((q >> 1) << 2) | (g & 1))) * 16) + (pp & 1) * 8);
        boff[0] = b0; boff[1] = b0 ^ 8u; boff[2] = b0 ^ 64u; boff[3] = b0 ^ 72u; }
    unsigned voff[8]; k1_voff(u0.type, wid, lane, voff);
    Frags f0, f1;
    const unsigned l0 = (unsigned)(size_t)lds;
    PfS pf; pf.m = l0 + 139264 + wid * 1024; pf.v = lane * 16; int pfg = 0; pf.s = k1_pfbase(p, pfg++, wid);
#define PF1() do { asm volatile("s_mov_b32 m0, %2\n\ts_nop 4\n\tglobal_load_lds_dwordx4 %0, %1" :: "v"(pf.v), "s"(pf.s), "s"(pf.m) : "memory", "m0"); pf.s += 4096; } while (0)
#define PF0_LOADS() do {} while (0)
#define STAGE4() do { \
    stage_all(u0, 0, voff, l0 + 0 * SLOT + wid * 4096, std::make_integer_sequence<int, 8>{}); PF1(); \
    stage_all(u0, 1, voff, l0 + 1 * SLOT + wid * 4096, std::make_integer_sequence<int, 8>{}); PF1(); \
    PF0_LOADS(); \
    stage_all(u0, 2, voff, l0 + 2 * SLOT + wid * 4096, std::make_integer_sequence<int, 8>{}); PF1(); \
    stage_all(u0, 3, voff, l0 + 3 * SLOT + wid * 4096, std::make_integer_sequence<int, 8>{}); PF1(); } while (0)
    if (s < 16) {
        u32x4 lnv[8];
        ln_load(lnv, p.x, xcd * 16 + s, wid, lane);
        STAGE4();
        ln_block(lnv, mkrs(p.ws + WS_XN, 4u << 20), (LAS float*)(lds + 131072), xcd * 16 + s, wid, lane);
        asm volatile("s_waitcnt vmcnt(0)" ::: "memory");
    } else { STAGE4(); asm volatile("s_waitcnt vmcnt(" PRW ")" ::: "memory"); }
    BAR;
    const unsigned tag = (unsigned)__builtin_amdgcn_readfirstlane(*(volatile LAS unsigned*)(lds + 133120 + wid * 256)) + 0x9E3779B9u;
    if (s < 16 && wid == 0 && lane == 0) flag_set(p.ws, W_XN + xcd * 16 + s, tag);
    if (wid == 0 && lane == 0) flag_set(p.ws, W_START + blockIdx.x, tag);
    { unsigned bad[4];
#pragma unroll
      for (int i = 0; i < 4; ++i) bad[i] = l0 + boff[i];
      read_all(f0, l0 + k1_aoff(u0.type, wr, lane), bad, std::make_integer_sequence<int, 8>{}); }
    STAMP(1);
    G4Addr ad; ad.a0 = ad.aX = l0 + k1_aoff(u0.type, wr, lane); ad.aY = ad.aX + 2 * SLOT; ad.ldsb = l0 + wid * 4096;
#pragma unroll
    for (int i = 0; i < 4; ++i) { ad.bX[i] = l0 + boff[i]; ad.bY[i] = l0 + boff[i] + 2 * SLOT; }
    const int nunits = has2 ? 2 : 1;
    Unit cur = u0; int pend = -1;
    Unit ua; k1_unit(ua, p, U_ATT, att_h, att_t1);
    for (int ui = 0; ui < nunits; ++ui) {
        const bool toatt = ui == 1;
        Unit nx = u1; if (toatt) nx = ua;
        const int pidx = toatt ? (lane < 16 ? W_KV + (att_h >> 1) * 64 + lane : -1) : (lane < 16 ? W_XN + 16 * u1.pm + lane : -1);
        g4_kloop(lds, p, pf, pfg, f0, f1, cur, nx, voff, ad, l0 + k1_aoff(u1.type, wr, lane), wid, lane, pend, tag, pidx, toatt ? 16 : (has2 ? 4 : -1), codep);
        PIN_FRAGS(f0);
        STAMP(2 + 2 * ui);
        k1_epi_T(cur, p, wr, wc, lane);
        STAMP(3 + 2 * ui);
        pend = unit_flag(cur);
        cur = u1;
    }
    DRAIN_BAR();
    if (wid == 0 && lane == 0) flag_set(p.ws, pend, tag);
    STAMP(6);
    return tag;
}

constexpr int ATT_LDS = 163840;
struct AttC { bf16x8 qf[4][4]; f32x4 negm[4]; unsigned vb[2], ka, kan, dvo[2]; float ninf; unsigned long long mask[4]; unsigned dm0[2]; const char* dsb[2]; const char* pfs[2]; };
DI bf16x8 mk8(unsigned a, unsigned b, unsigned c, unsigned d) { return __builtin_bit_cast(bf16x8, (u32x4){a, b, c, d}); }
DI void att_half_0n(AttC& c) {
    f32x4 S[4][2]; float E[4][8]; unsigned PW[4][4];
    asm volatile("s_waitcnt vmcnt(10) lgkmcnt(0)\n\t"
        "s_barrier\n\t"
        "v_mfma_f32_16x16x32_bf16 %[S00], a[144:147], %[q00], %[negm0]\n\t"
        "ds_read_b64_tr_b16 a[176:177], %[vb0] offset:0\n\t"
        "ds_read_b64_tr_b16 a[178:179], %[vb0] offset:4096\n\t"
        "v_mfma_f32_16x16x32_bf16 %[S01], a[160:163], %[q00], %[negm0]\n\t"
        "ds_read_b64_tr_b16 a[180:181], %[vb1] offset:0\n\t"
        "ds_read_b64_tr_b16 a[182:183], %[vb1] offset:4096\n\t"
        "v_mfma_f32_16x16x32_bf16 %[S00], a[148:151], %[q01], %[S00]\n\t"
        "ds_read_b64_tr_b16 a[184:185], %[vb0] offset:1024\n\t"
        "ds_read_b64_tr_b16 a[186:187], %[vb0] offset:5120\n\t"
        "v_mfma_f32_16x16x32_bf16 %[S01], a[164:167], %[q01], %[S01]\n\t"
        "s_add_u32 m0, %[dm], 0\n\t"
        "s_nop 0\n\t"
        "global_load_lds_dwordx4 %[dvo0], %[dsb] offset:0\n\t"
        "v_mfma_f32_16x16x32_bf16 %[S00], a[152:155], %[q02], %[S00]\n\t"
        "ds_read_b64_tr_b16 a[188:189], %[vb1] offset:1024\n\t"
        "ds_read_b64_tr_b16 a[190:191], %[vb1] offset:5120\n\t"
        "v_mfma_f32_16x16x32_bf16 %[S01], a[168:171], %[q02], %[S01]\n\t"
        "ds_read_b64_tr_b16 a[192:193], %[vb0] offset:2048\n\t"
        "ds_read_b64_tr_b16 a[194:195], %[vb0] offset:6144\n\t"
        "v_mfma_f32_16x16x32_bf16 %[S00], a[156:159], %[q03], %[S00]\n\t"
        "ds_read_b64_tr_b16 a[196:197], %[vb1] offset:2048\n\t"
        "ds_read_b64_tr_b16 a[198:199], %[vb1] offset:6144\n\t"
        "v_mfma_f32_16x16x32_bf16 %[S01], a[172:175], %[q03], %[S01]\n\t"
        "ds_read_b64_tr_b16 a[200:201], %[vb0] offset:3072\n\t"
        "ds_read_b64_tr_b16 a[202:203], %[vb0] offset:7168"
        : [S00] "=&v"(S[0][0]), [S01] "=&v"(S[0][1])
        : [q00] "v"(c.qf[0][0]), [negm0] "v"(c.negm[0]), [vb0] "v"(c.vb[0]), [vb1] "v"(c.vb[1]), [q01] "v"(c.qf[0][1]), [dm] "s"(c.dm0[0]), [dvo0] "v"(c.dvo[0]), [dsb] "s"(c.dsb[0]), [q02] "v"(c.qf[0][2]), [q03] "v"(c.qf[0][3])
        : "memory", "m0", "scc", ACLOB);
    asm volatile("v_mfma_f32_16x16x32_bf16 %[S10], a[144:147], %[q10], %[negm1]\n\t"
        "ds_read_b64_tr_b16 a[204:205], %[vb1] offset:3072\n\t"
        "ds_read_b64_tr_b16 a[206:207], %[vb1] offset:7168\n\t"
        "v_mfma_f32_16x16x32_bf16 %[S11], a[160:163], %[q10], %[negm1]\n\t"
        "v_mfma_f32_16x16x32_bf16 %[S10], a[148:151], %[q11], %[S10]\n\t"
        "v_exp_f32_e32 %[e00], %[s000]\n\t"
        "v_mfma_f32_16x16x32_bf16 %[S11], a[164:167], %[q11], %[S11]\n\t"
        "global_load_lds_dwordx4 %[dvo0], %[dsb] offset:1024\n\t"
        "v_mfma_f32_16x16x32_bf16 %[S10], a[152:155], %[q12], %[S10]\n\t"
        "v_exp_f32_e32 %[e01], %[s001]\n\t"
        "v_mfma_f32_16x16x32_bf16 %[S11], a[168:171], %[q12], %[S11]\n\t"
        "v_exp_f32_e32 %[e02], %[s002]\n\t"
        "v_mfma_f32_16x16x32_bf16 %[S10], a[156:159], %[q13], %[S10]\n\t"
        "v_exp_f32_e32 %[e03], %[s003]\n\t"
        "v_mfma_f32_16x16x32_bf16 %[S11], a[172:175], %[q13], %[S11]\n\t"
        "v_exp_f32_e32 %[e04], %[s010]"
        : [S10] "=&v"(S[1][0]), [S11] "=&v"(S[1][1]), [e00] "=&v"(E[0][0]), [e01] "=&v"(E[0][1]), [e02] "=&v"(E[0][2]), [e03] "=&v"(E[0][3]), [e04] "=&v"(E[0][4])
        : [q10] "v"(c.qf[1][0]), [negm1] "v"(c.negm[1]), [vb1] "v"(c.vb[1]), [q11] "v"(c.qf[1][1]), [s000] "v"(S[0][0][0]), [dvo0] "v"(c.dvo[0]), [dsb] "s"(c.dsb[0]), [q12] "v"(c.qf[1][2]), [s001] "v"(S[0][0][1]), [s002] "v"(S[0][0][2]), [q13] "v"(c.qf[1][3]), [s003] "v"(S[0][0][3]), [s010] "v"(S[0][1][0])
        : "memory", "m0", "scc", ACLOB);
    asm volatile("v_mfma_f32_16x16x32_bf16 %[S20], a[144:147], %[q20], %[negm2]\n\t"
        "v_exp_f32_e32 %[e05], %[s011]\n\t"
        "v_mfma_f32_16x16x32_bf16 %[S21], a[160:163], %[q20], %[negm2]\n\t"
        "v_exp_f32_e32 %[e06], %[s012]\n\t"
        "v_mfma_f32_16x16x32_bf16 %[S20], a[148:151], %[q21], %[S20]\n\t"
        "v_exp_f32_e32 %[e07], %[s013]\n\t"
        "v_mfma_f32_16x16x32_bf16 %[S21], a[164:167], %[q21], %[S21]\n\t"
        "global_load_lds_dwordx4 %[dvo0], %[dsb] offset:2048\n\t"
        "v_mfma_f32_16x16x32_bf16 %[S20], a[152:155], %[q22], %[S20]\n\t"
        "v_cvt_pk_bf16_f32 %[pw00], %[e00], %[e01]\n\t"
        "v_cvt_pk_bf16_f32 %[pw01], %[e02], %[e03]\n\t"
        "v_mfma_f32_16x16x32_bf16 %[S21], a[168:171], %[q22], %[S21]\n\t"
        "v_cvt_pk_bf16_f32 %[pw02], %[e04], %[e05]\n\t"
        "v_cvt_pk_bf16_f32 %[pw03], %[e06], %[e07]\n\t"
        "v_mfma_f32_16x16x32_bf16 %[S20], a[156:159], %[q23], %[S20]\n\t"
        "v_exp_f32_e32 %[e10], %[s100]\n\t"
        "v_mfma_f32_16x16x32_bf16 %[S21], a[172:175], %[q23], %[S21]\n\t"
        "v_exp_f32_e32 %[e11], %[s101]"
        : [S20] "=&v"(S[2][0]), [e05] "=&v"(E[0][5]), [S21] "=&v"(S[2][1]), [e06] "=&v"(E[0][6]), [e07] "=&v"(E[0][7]), [pw00] "=&v"(PW[0][0]), [pw01] "=&v"(PW[0][1]), [pw02] "=&v"(PW[0][2]), [pw03] "=&v"(PW[0][3]), [e10] "=&v"(E[1][0]), [e11] "=&v"(E[1][1])
        : [q20] "v"(c.qf[2][0]), [negm2] "v"(c.negm[2]), [s011] "v"(S[0][1][1]), [s012] "v"(S[0][1][2]), [q21] "v"(c.qf[2][1]), [s013] "v"(S[0][1][3]), [dvo0] "v"(c.dvo[0]), [dsb] "s"(c.dsb[0]), [q22] "v"(c.qf[2][2]), [e00] "v"(E[0][0]), [e01] "v"(E[0][1]), [e02] "v"(E[0][2]), [e03] "v"(E[0][3]), [e04] "v"(E[0][4]), [q23] "v"(c.qf[2][3]), [s100] "v"(S[1][0][0]), [s101] "v"(S[1][0][1])
        : "memory", "m0", "scc", ACLOB);
    asm volatile("v_mfma_f32_16x16x32_bf16 %[S30], a[144:147], %[q30], %[negm3]\n\t"
        "v_exp_f32_e32 %[e12], %[s102]\n\t"
        "v_mfma_f32_16x16x32_bf16 %[S31], a[160:163], %[q30], %[negm3]\n\t"
        "v_exp_f32_e32 %[e13], %[s103]\n\t"
        "v_mfma_f32_16x16x32_bf16 %[S30], a[148:151], %[q31], %[S30]\n\t"
        "v_exp_f32_e32 %[e14], %[s110]\n\t"
        "v_mfma_f32_16x16x32_bf16 %[S31], a[164:167], %[q31], %[S31]\n\t"
        "global_load_lds_dwordx4 %[dvo0], %[dsb] offset:3072\n\t"
        "v_mfma_f32_16x16x32_bf16 %[S30], a[152:155], %[q32], %[S30]\n\t"
        "v_exp_f32_e32 %[e15], %[s111]\n\t"
        "v_mfma_f32_16x16x32_bf16 %[S31], a[168:171], %[q32], %[S31]\n\t"
        "v_exp_f32_e32 %[e16], %[s112]\n\t"
        "v_mfma_f32_16x16x32_bf16 %[S30], a[156:159], %[q33], %[S30]\n\t"
        "v_exp_f32_e32 %[e17], %[s113]\n\t"
        "v_mfma_f32_16x16x32_bf16 %[S31], a[172:175], %[q33], %[S31]\n\t"
        "v_cvt_pk_bf16_f32 %[pw10], %[e10], %[e11]\n\t"
        "v_cvt_pk_bf16_f32 %[pw11], %[e12], %[e13]"
        : [S30] "=&v"(S[3][0]), [e12] "=&v"(E[1][2]), [S31] "=&v"(S[3][1]), [e13] "=&v"(E[1][3]), [e14] "=&v"(E[1][4]), [e15] "=&v"(E[1][5]), [e16] "=&v"(E[1][6]), [e17] "=&v"(E[1][7]), [pw10] "=&v"(PW[1][0]), [pw11] "=&v"(PW[1][1])
        : [q30] "v"(c.qf[3][0]), [negm3] "v"(c.negm[3]), [s102] "v"(S[1][0][2]), [s103] "v"(S[1][0][3]), [q31] "v"(c.qf[3][1]), [s110] "v"(S[1][1][0]), [dvo0] "v"(c.dvo[0]), [dsb] "s"(c.dsb[0]), [q32] "v"(c.qf[3][2]), [s111] "v"(S[1][1][1]), [s112] "v"(S[1][1][2]), [q33] "v"(c.qf[3][3]), [s113] "v"(S[1][1][3]), [e10] "v"(E[1][0]), [e11] "v"(E[1][1])
        : "memory", "m0", "scc", ACLOB);
    asm volatile("s_waitcnt lgkmcnt(0)\n\t"
        "s_nop 1\n\t"
        "v_mfma_f32_16x16x32_bf16 a[0:3], a[176:179], %[p0], a[0:3]\n\t"
        "v_cvt_pk_bf16_f32 %[pw12], %[e14], %[e15]\n\t"
        "v_cvt_pk_bf16_f32 %[pw13], %[e16], %[e17]\n\t"
        "v_mfma_f32_16x16x32_bf16 a[16:19], a[180:183], %[p0], a[16:19]\n\t"
        "v_exp_f32_e32 %[e20], %[s200]\n\t"
        "v_mfma_f32_16x16x32_bf16 a[32:35], a[184:187], %[p0], a[32:35]\n\t"
        "v_exp_f32_e32 %[e21], %[s201]\n\t"
        "v_mfma_f32_16x16x32_bf16 a[48:51], a[188:191], %[p0], a[48:51]\n\t"
        "s_add_u32 m0, %[dm], 4096\n\t"
        "s_nop 0\n\t"
        "global_load_lds_dwordx4 %[dvo1], %[dsb] offset:0\n\t"
        "v_mfma_f32_16x16x32_bf16 a[64:67], a[192:195], %[p0], a[64:67]\n\t"
        "v_exp_f32_e32 %[e22], %[s202]\n\t"
        "v_mfma_f32_16x16x32_bf16 a[80:83], a[196:199], %[p0], a[80:83]\n\t"
        "v_exp_f32_e32 %[e23], %[s203]\n\t"
        "v_mfma_f32_16x16x32_bf16 a[96:99], a[200:203], %[p0], a[96:99]\n\t"
        "v_exp_f32_e32 %[e24], %[s210]\n\t"
        "v_mfma_f32_16x16x32_bf16 a[112:115], a[204:207], %[p0], a[112:115]\n\t"
        "v_exp_f32_e32 %[e25], %[s211]\n\t"
        "v_mfma_f32_16x16x32_bf16 a[128:131], a[208:211], %[p0], a[128:131]\n\t"
        "v_exp_f32_e32 %[e26], %[s212]"
        : [pw12] "=&v"(PW[1][2]), [pw13] "=&v"(PW[1][3]), [e20] "=&v"(E[2][0]), [e21] "=&v"(E[2][1]), [e22] "=&v"(E[2][2]), [e23] "=&v"(E[2][3]), [e24] "=&v"(E[2][4]), [e25] "=&v"(E[2][5]), [e26] "=&v"(E[2][6])
        : [p0] "v"(mk8(PW[0][0], PW[0][1], PW[0][2], PW[0][3])), [e14] "v"(E[1][4]), [e15] "v"(E[1][5]), [e16] "v"(E[1][6]), [e17] "v"(E[1][7]), [s200] "v"(S[2][0][0]), [s201] "v"(S[2][0][1]), [dm] "s"(c.dm0[0]), [dvo1] "v"(c.dvo[1]), [dsb] "s"(c.dsb[0]), [s202] "v"(S[2][0][2]), [s203] "v"(S[2][0][3]), [s210] "v"(S[2][1][0]), [s211] "v"(S[2][1][1]), [s212] "v"(S[2][1][2])
        : "memory", "m0", "scc", ACLOB);
    asm volatile("s_nop 1\n\t"
        "v_mfma_f32_16x16x32_bf16 a[4:7], a[176:179], %[p1], a[4:7]\n\t"
        "v_exp_f32_e32 %[e27], %[s213]\n\t"
        "v_mfma_f32_16x16x32_bf16 a[20:23], a[180:183], %[p1], a[20:23]\n\t"
        "v_cvt_pk_bf16_f32 %[pw20], %[e20], %[e21]\n\t"
        "v_cvt_pk_bf16_f32 %[pw21], %[e22], %[e23]\n\t"
        "v_mfma_f32_16x16x32_bf16 a[36:39], a[184:187], %[p1], a[36:39]\n\t"
        "v_cvt_pk_bf16_f32 %[pw22], %[e24], %[e25]\n\t"
        "v_cvt_pk_bf16_f32 %[pw23], %[e26], %[e27]\n\t"
        "v_mfma_f32_16x16x32_bf16 a[52:55], a[188:191], %[p1], a[52:55]\n\t"
        "global_load_lds_dwordx4 %[dvo1], %[dsb] offset:1024\n\t"
        "v_mfma_f32_16x16x32_bf16 a[68:71], a[192:195], %[p1], a[68:71]\n\t"
        "v_exp_f32_e32 %[e30], %[s300]\n\t"
        "v_mfma_f32_16x16x32_bf16 a[84:87], a[196:199], %[p1], a[84:87]\n\t"
        "v_exp_f32_e32 %[e31], %[s301]\n\t"
        "v_mfma_f32_16x16x32_bf16 a[100:103], a[200:203], %[p1], a[100:103]\n\t"
        "v_exp_f32_e32 %[e32], %[s302]\n\t"
        "v_mfma_f32_16x16x32_bf16 a[116:119], a[204:207], %[p1], a[116:119]\n\t"
        "v_exp_f32_e32 %[e33], %[s303]\n\t"
        "v_mfma_f32_16x16x32_bf16 a[132:135], a[208:211], %[p1], a[132:135]\n\t"
        "v_exp_f32_e32 %[e34], %[s310]"
        : [e27] "=&v"(E[2][7]), [pw20] "=&v"(PW[2][0]), [pw21] "=&v"(PW[2][1]), [pw22] "=&v"(PW[2][2]), [pw23] "=&v"(PW[2][3]), [e30] "=&v"(E[3][0]), [e31] "=&v"(E[3][1]), [e32] "=&v"(E[3][2]), [e33] "=&v"(E[3][3]), [e34] "=&v"(E[3][4])
        : [p1] "v"(mk8(PW[1][0], PW[1][1], PW[1][2], PW[1][3])), [s213] "v"(S[2][1][3]), [e20] "v"(E[2][0]), [e21] "v"(E[2][1]), [e22] "v"(E[2][2]), [e23] "v"(E[2][3]), [e24] "v"(E[2][4]), [e25] "v"(E[2][5]), [e26] "v"(E[2][6]), [dvo1] "v"(c.dvo[1]), [dsb] "s"(c.dsb[0]), [s300] "v"(S[3][0][0]), [s301] "v"(S[3][0][1]), [s302] "v"(S[3][0][2]), [s303] "v"(S[3][0][3]), [s310] "v"(S[3][1][0])
        : "memory", "m0", "scc", ACLOB);
    asm volatile("s_nop 1\n\t"
        "v_mfma_f32_16x16x32_bf16 a[8:11], a[176:179], %[p2], a[8:11]\n\t"
        "v_exp_f32_e32 %[e35], %[s311]\n\t"
        "v_mfma_f32_16x16x32_bf16 a[24:27], a[180:183], %[p2], a[24:27]\n\t"
        "v_exp_f32_e32 %[e36], %[s312]\n\t"
        "v_mfma_f32_16x16x32_bf16 a[40:43], a[184:187], %[p2], a[40:43]\n\t"
        "v_exp_f32_e32 %[e37], %[s313]\n\t"
        "v_mfma_f32_16x16x32_bf16 a[56:59], a[188:191], %[p2], a[56:59]\n\t"
        "global_load_lds_dwordx4 %[dvo1], %[dsb] offset:2048\n\t"
        "v_mfma_f32_16x16x32_bf16 a[72:75], a[192:195], %[p2], a[72:75]\n\t"
        "v_cvt_pk_bf16_f32 %[pw30], %[e30], %[e31]\n\t"
        "v_cvt_pk_bf16_f32 %[pw31], %[e32], %[e33]\n\t"
        "v_mfma_f32_16x16x32_bf16 a[88:91], a[196:199], %[p2], a[88:91]\n\t"
        "v_cvt_pk_bf16_f32 %[pw32], %[e34], %[e35]\n\t"
        "v_cvt_pk_bf16_f32 %[pw33], %[e36], %[e37]\n\t"
        "v_mfma_f32_16x16x32_bf16 a[104:107], a[200:203], %[p2], a[104:107]\n\t"
        "ds_read_b128 a[144:147], %[ka] offset:8192\n\t"
        "ds_read_b128 a[148:151], %[ka] offset:9216\n\t"
        "v_mfma_f32_16x16x32_bf16 a[120:123], a[204:207], %[p2], a[120:123]\n\t"
        "ds_read_b128 a[152:155], %[ka] offset:10240\n\t"
        "ds_read_b128 a[156:159], %[ka] offset:11264\n\t"
        "v_mfma_f32_16x16x32_bf16 a[136:139], a[208:211], %[p2], a[136:139]\n\t"
        "ds_read_b128 a[160:163], %[ka] offset:12288\n\t"
        "ds_read_b128 a[164:167], %[ka] offset:13312"
        : [e35] "=&v"(E[3][5]), [e36] "=&v"(E[3][6]), [e37] "=&v"(E[3][7]), [pw30] "=&v"(PW[3][0]), [pw31] "=&v"(PW[3][1]), [pw32] "=&v"(PW[3][2]), [pw33] "=&v"(PW[3][3])
        : [p2] "v"(mk8(PW[2][0], PW[2][1], PW[2][2], PW[2][3])), [s311] "v"(S[3][1][1]), [s312] "v"(S[3][1][2]), [s313] "v"(S[3][1][3]), [dvo1] "v"(c.dvo[1]), [dsb] "s"(c.dsb[0]), [e30] "v"(E[3][0]), [e31] "v"(E[3][1]), [e32] "v"(E[3][2]), [e33] "v"(E[3][3]), [e34] "v"(E[3][4]), [ka] "v"(c.ka)
        : "memory", "m0", "scc", ACLOB);
    asm volatile("s_nop 1\n\t"
        "v_mfma_f32_16x16x32_bf16 a[12:15], a[176:179], %[p3], a[12:15]\n\t"
        "ds_read_b128 a[168:171], %[ka] offset:14336\n\t"
        "ds_read_b128 a[172:175], %[ka] offset:15360\n\t"
        "v_mfma_f32_16x16x32_bf16 a[28:31], a[180:183], %[p3], a[28:31]\n\t"
        "v_mfma_f32_16x16x32_bf16 a[44:47], a[184:187], %[p3], a[44:47]\n\t"
        "v_mfma_f32_16x16x32_bf16 a[60:63], a[188:191], %[p3], a[60:63]\n\t"
        "global_load_lds_dwordx4 %[dvo1], %[dsb] offset:3072\n\t"
        "global_load_dwordx4 a[252:255], %[dvo0], %[pfs0]\n\t"
        "v_mfma_f32_16x16x32_bf16 a[76:79], a[192:195], %[p3], a[76:79]\n\t"
        "v_mfma_f32_16x16x32_bf16 a[92:95], a[196:199], %[p3], a[92:95]\n\t"
        "v_mfma_f32_16x16x32_bf16 a[108:111], a[200:203], %[p3], a[108:111]\n\t"
        "v_mfma_f32_16x16x32_bf16 a[124:127], a[204:207], %[p3], a[124:127]\n\t"
        "v_mfma_f32_16x16x32_bf16 a[140:143], a[208:211], %[p3], a[140:143]"
        :
        : [p3] "v"(mk8(PW[3][0], PW[3][1], PW[3][2], PW[3][3])), [ka] "v"(c.ka), [dvo1] "v"(c.dvo[1]), [dsb] "s"(c.dsb[0]), [dvo0] "v"(c.dvo[0]), [pfs0] "s"(c.pfs[0])
        : "memory", "m0", "scc", ACLOB);
}
DI void att_half_0d(AttC& c) {
    f32x4 S[4][2]; float E[4][8]; unsigned PW[4][4];
    asm volatile("s_waitcnt vmcnt(10) lgkmcnt(0)\n\t"
        "s_barrier\n\t"
        "v_mfma_f32_16x16x32_bf16 %[S00], a[144:147], %[q00], %[negm0]\n\t"
        "ds_read_b64_tr_b16 a[176:177], %[vb0] offset:0\n\t"
        "ds_read_b64_tr_b16 a[178:179], %[vb0] offset:4096\n\t"
        "v_mfma_f32_16x16x32_bf16 %[S00], a[148:151], %[q01], %[S00]\n\t"
        "ds_read_b64_tr_b16 a[180:181], %[vb1] offset:0\n\t"
        "ds_read_b64_tr_b16 a[182:183], %[vb1] offset:4096\n\t"
        "v_mfma_f32_16x16x32_bf16 %[S00], a[152:155], %[q02], %[S00]\n\t"
        "ds_read_b64_tr_b16 a[184:185], %[vb0] offset:1024\n\t"
        "ds_read_b64_tr_b16 a[186:187], %[vb0] offset:5120\n\t"
        "v_mfma_f32_16x16x32_bf16 %[S00], a[156:159], %[q03], %[S00]\n\t"
        "s_add_u32 m0, %[dm], 0\n\t"
        "s_nop 0\n\t"
        "global_load_lds_dwordx4 %[dvo0], %[dsb] offset:0\n\t"
        "s_nop 7"
        : [S00] "=&v"(S[0][0])
        : [q00] "v"(c.qf[0][0]), [negm0] "v"(c.negm[0]), [vb0] "v"(c.vb[0]), [q01] "v"(c.qf[0][1]), [vb1] "v"(c.vb[1]), [q02] "v"(c.qf[0][2]), [q03] "v"(c.qf[0][3]), [dm] "s"(c.dm0[0]), [dvo0] "v"(c.dvo[0]), [dsb] "s"(c.dsb[0])
        : "memory", "m0", "scc", ACLOB);
    asm volatile("v_mfma_f32_16x16x32_bf16 %[S10], a[144:147], %[q10], %[negm1]\n\t"
        "ds_read_b64_tr_b16 a[188:189], %[vb1] offset:1024\n\t"
        "ds_read_b64_tr_b16 a[190:191], %[vb1] offset:5120\n\t"
        "v_mfma_f32_16x16x32_bf16 %[S11], a[160:163], %[q10], %[negm1]\n\t"
        "ds_read_b64_tr_b16 a[192:193], %[vb0] offset:2048\n\t"
        "ds_read_b64_tr_b16 a[194:195], %[vb0] offset:6144\n\t"
        "v_mfma_f32_16x16x32_bf16 %[S10], a[148:151], %[q11], %[S10]\n\t"
        "v_cndmask_b32_e64 %[e00], %[s000], %[ninf], %[mk0]\n\t"
        "v_exp_f32_e32 %[e00], %[e00]\n\t"
        "v_mfma_f32_16x16x32_bf16 %[S11], a[164:167], %[q11], %[S11]\n\t"
        "global_load_lds_dwordx4 %[dvo0], %[dsb] offset:1024\n\t"
        "v_mfma_f32_16x16x32_bf16 %[S10], a[152:155], %[q12], %[S10]\n\t"
        "v_cndmask_b32_e64 %[e01], %[s001], %[ninf], %[mk1]\n\t"
        "v_exp_f32_e32 %[e01], %[e01]\n\t"
        "v_mfma_f32_16x16x32_bf16 %[S11], a[168:171], %[q12], %[S11]\n\t"
        "v_cndmask_b32_e64 %[e02], %[s002], %[ninf], %[mk2]\n\t"
        "v_exp_f32_e32 %[e02], %[e02]\n\t"
        "v_mfma_f32_16x16x32_bf16 %[S10], a[156:159], %[q13], %[S10]\n\t"
        "v_cndmask_b32_e64 %[e03], %[s003], %[ninf], %[mk3]\n\t"
        "v_exp_f32_e32 %[e03], %[e03]\n\t"
        "v_mfma_f32_16x16x32_bf16 %[S11], a[172:175], %[q13], %[S11]\n\t"
        "v_cvt_pk_bf16_f32 %[pw00], %[e00], %[e01]\n\t"
        "v_cvt_pk_bf16_f32 %[pw01], %[e02], %[e03]"
        : [S10] "=&v"(S[1][0]), [S11] "=&v"(S[1][1]), [e00] "=&v"(E[0][0]), [e01] "=&v"(E[0][1]), [e02] "=&v"(E[0][2]), [e03] "=&v"(E[0][3]), [pw00] "=&v"(PW[0][0]), [pw01] "=&v"(PW[0][1])
        : [q10] "v"(c.qf[1][0]), [negm1] "v"(c.negm[1]), [vb1] "v"(c.vb[1]), [vb0] "v"(c.vb[0]), [q11] "v"(c.qf[1][1]), [s000] "v"(S[0][0][0]), [ninf] "v"(c.ninf), [mk0] "s"(c.mask[0]), [dvo0] "v"(c.dvo[0]), [dsb] "s"(c.dsb[0]), [q12] "v"(c.qf[1][2]), [s001] "v"(S[0][0][1]), [mk1] "s"(c.mask[1]), [s002] "v"(S[0][0][2]), [mk2] "s"(c.mask[2]), [q13] "v"(c.qf[1][3]), [s003] "v"(S[0][0][3]), [mk3] "s"(c.mask[3])
        : "memory", "m0", "scc", ACLOB);
    asm volatile("v_mfma_f32_16x16x32_bf16 %[S20], a[144:147], %[q20], %[negm2]\n\t"
        "v_mov_b32_e32 %[pw02], 0\n\t"
        "v_mov_b32_e32 %[pw03], 0\n\t"
        "v_mfma_f32_16x16x32_bf16 %[S21], a[160:163], %[q20], %[negm2]\n\t"
        "ds_read_b64_tr_b16 a[196:197], %[vb1] offset:2048\n\t"
        "ds_read_b64_tr_b16 a[198:199], %[vb1] offset:6144\n\t"
        "v_mfma_f32_16x16x32_bf16 %[S20], a[148:151], %[q21], %[S20]\n\t"
        "v_exp_f32_e32 %[e10], %[s100]\n\t"
        "v_mfma_f32_16x16x32_bf16 %[S21], a[164:167], %[q21], %[S21]\n\t"
        "global_load_lds_dwordx4 %[dvo0], %[dsb] offset:2048\n\t"
        "v_mfma_f32_16x16x32_bf16 %[S20], a[152:155], %[q22], %[S20]\n\t"
        "v_exp_f32_e32 %[e11], %[s101]\n\t"
        "v_mfma_f32_16x16x32_bf16 %[S21], a[168:171], %[q22], %[S21]\n\t"
        "v_exp_f32_e32 %[e12], %[s102]\n\t"
        "v_mfma_f32_16x16x32_bf16 %[S20], a[156:159], %[q23], %[S20]\n\t"
        "v_exp_f32_e32 %[e13], %[s103]\n\t"
        "v_mfma_f32_16x16x32_bf16 %[S21], a[172:175], %[q23], %[S21]\n\t"
        "v_cndmask_b32_e64 %[e14], %[s110], %[ninf], %[mk0]\n\t"
        "v_exp_f32_e32 %[e14], %[e14]"
        : [S20] "=&v"(S[2][0]), [pw02] "=&v"(PW[0][2]), [pw03] "=&v"(PW[0][3]), [S21] "=&v"(S[2][1]), [e10] "=&v"(E[1][0]), [e11] "=&v"(E[1][1]), [e12] "=&v"(E[1][2]), [e13] "=&v"(E[1][3]), [e14] "=&v"(E[1][4])
        : [q20] "v"(c.qf[2][0]), [negm2] "v"(c.negm[2]), [vb1] "v"(c.vb[1]), [q21] "v"(c.qf[2][1]), [s100] "v"(S[1][0][0]), [dvo0] "v"(c.dvo[0]), [dsb] "s"(c.dsb[0]), [q22] "v"(c.qf[2][2]), [s101] "v"(S[1][0][1]), [s102] "v"(S[1][0][2]), [q23] "v"(c.qf[2][3]), [s103] "v"(S[1][0][3]), [s110] "v"(S[1][1][0]), [ninf] "v"(c.ninf), [mk0] "s"(c.mask[0])
        : "memory", "m0", "scc", ACLOB);
    asm volatile("v_mfma_f32_16x16x32_bf16 %[S30], a[144:147], %[q30], %[negm3]\n\t"
        "v_cndmask_b32_e64 %[e15], %[s111], %[ninf], %[mk1]\n\t"
        "v_exp_f32_e32 %[e15], %[e15]\n\t"
        "v_mfma_f32_16x16x32_bf16 %[S31], a[160:163], %[q30], %[negm3]\n\t"
        "v_cndmask_b32_e64 %[e16], %[s112], %[ninf], %[mk2]\n\t"
        "v_exp_f32_e32 %[e16], %[e16]\n\t"
        "v_mfma_f32_16x16x32_bf16 %[S30], a[148:151], %[q31], %[S30]\n\t"
        "v_cndmask_b32_e64 %[e17], %[s113], %[ninf], %[mk3]\n\t"
        "v_exp_f32_e32 %[e17], %[e17]\n\t"
        "v_mfma_f32_16x16x32_bf16 %[S31], a[164:167], %[q31], %[S31]\n\t"
        "global_load_lds_dwordx4 %[dvo0], %[dsb] offset:3072\n\t"
        "v_mfma_f32_16x16x32_bf16 %[S30], a[152:155], %[q32], %[S30]\n\t"
        "v_cvt_pk_bf16_f32 %[pw10], %[e10], %[e11]\n\t"
        "v_cvt_pk_bf16_f32 %[pw11], %[e12], %[e13]\n\t"
        "v_mfma_f32_16x16x32_bf16 %[S31], a[168:171], %[q32], %[S31]\n\t"
        "v_cvt_pk_bf16_f32 %[pw12], %[e14], %[e15]\n\t"
        "v_cvt_pk_bf16_f32 %[pw13], %[e16], %[e17]\n\t"
        "v_mfma_f32_16x16x32_bf16 %[S30], a[156:159], %[q33], %[S30]\n\t"
        "v_exp_f32_e32 %[e20], %[s200]\n\t"
        "v_mfma_f32_16x16x32_bf16 %[S31], a[172:175], %[q33], %[S31]"
        : [S30] "=&v"(S[3][0]), [e15] "=&v"(E[1][5]), [S31] "=&v"(S[3][1]), [e16] "=&v"(E[1][6]), [e17] "=&v"(E[1][7]), [pw10] "=&v"(PW[1][0]), [pw11] "=&v"(PW[1][1]), [pw12] "=&v"(PW[1][2]), [pw13] "=&v"(PW[1][3]), [e20] "=&v"(E[2][0])
        : [q30] "v"(c.qf[3][0]), [negm3] "v"(c.negm[3]), [s111] "v"(S[1][1][1]), [ninf] "v"(c.ninf), [mk1] "s"(c.mask[1]), [s112] "v"(S[1][1][2]), [mk2] "s"(c.mask[2]), [q31] "v"(c.qf[3][1]), [s113] "v"(S[1][1][3]), [mk3] "s"(c.mask[3]), [dvo0] "v"(c.dvo[0]), [dsb] "s"(c.dsb[0]), [q32] "v"(c.qf[3][2]), [e10] "v"(E[1][0]), [e11] "v"(E[1][1]), [e12] "v"(E[1][2]), [e13] "v"(E[1][3]), [e14] "v"(E[1][4]), [q33] "v"(c.qf[3][3]), [s200] "v"(S[2][0][0])
        : "memory", "m0", "scc", ACLOB);
    asm volatile("v_exp_f32_e32 %[e21], %[s201]\n\t"
        "ds_read_b64_tr_b16 a[200:201], %[vb0] offset:3072\n\t"
        "ds_read_b64_tr_b16 a[202:203], %[vb0] offset:7168\n\t"
        "ds_read_b64_tr_b16 a[204:205], %[vb1] offset:3072\n\t"
        "ds_read_b64_tr_b16 a[206:207], %[vb1] offset:7168"
        : [e21] "=&v"(E[2][1])
        : [s201] "v"(S[2][0][1]), [vb0] "v"(c.vb[0]), [vb1] "v"(c.vb[1])
        : "memory", "m0", "scc", ACLOB);
    asm volatile("s_waitcnt lgkmcnt(0)\n\t"
        "s_nop 1\n\t"
        "v_mfma_f32_16x16x32_bf16 a[0:3], a[176:179], %[p0], a[0:3]\n\t"
        "v_exp_f32_e32 %[e22], %[s202]\n\t"
        "v_mfma_f32_16x16x32_bf16 a[16:19], a[180:183], %[p0], a[16:19]\n\t"
        "v_exp_f32_e32 %[e23], %[s203]\n\t"
        "v_mfma_f32_16x16x32_bf16 a[32:35], a[184:187], %[p0], a[32:35]\n\t"
        "v_exp_f32_e32 %[e24], %[s210]\n\t"
        "v_mfma_f32_16x16x32_bf16 a[48:51], a[188:191], %[p0], a[48:51]\n\t"
        "s_add_u32 m0, %[dm], 4096\n\t"
        "s_nop 0\n\t"
        "global_load_lds_dwordx4 %[dvo1], %[dsb] offset:0\n\t"
        "v_mfma_f32_16x16x32_bf16 a[64:67], a[192:195], %[p0], a[64:67]\n\t"
        "v_exp_f32_e32 %[e25], %[s211]\n\t"
        "v_mfma_f32_16x16x32_bf16 a[80:83], a[196:199], %[p0], a[80:83]\n\t"
        "v_exp_f32_e32 %[e26], %[s212]\n\t"
        "v_mfma_f32_16x16x32_bf16 a[96:99], a[200:203], %[p0], a[96:99]\n\t"
        "v_exp_f32_e32 %[e27], %[s213]\n\t"
        "v_mfma_f32_16x16x32_bf16 a[112:115], a[204:207], %[p0], a[112:115]\n\t"
        "v_cvt_pk_bf16_f32 %[pw20], %[e20], %[e21]\n\t"
        "v_cvt_pk_bf16_f32 %[pw21], %[e22], %[e23]\n\t"
        "v_mfma_f32_16x16x32_bf16 a[128:131], a[208:211], %[p0], a[128:131]\n\t"
        "v_cvt_pk_bf16_f32 %[pw22], %[e24], %[e25]\n\t"
        "v_cvt_pk_bf16_f32 %[pw23], %[e26], %[e27]"
        : [e22] "=&v"(E[2][2]), [e23] "=&v"(E[2][3]), [e24] "=&v"(E[2][4]), [e25] "=&v"(E[2][5]), [e26] "=&v"(E[2][6]), [e27] "=&v"(E[2][7]), [pw20] "=&v"(PW[2][0]), [pw21] "=&v"(PW[2][1]), [pw22] "=&v"(PW[2][2]), [pw23] "=&v"(PW[2][3])
        : [p0] "v"(mk8(PW[0][0], PW[0][1], PW[0][2], PW[0][3])), [s202] "v"(S[2][0][2]), [s203] "v"(S[2][0][3]), [s210] "v"(S[2][1][0]), [dm] "s"(c.dm0[0]), [dvo1] "v"(c.dvo[1]), [dsb] "s"(c.dsb[0]), [s211] "v"(S[2][1][1]), [s212] "v"(S[2][1][2]), [s213] "v"(S[2][1][3]), [e20] "v"(E[2][0]), [e21] "v"(E[2][1])
        : "memory", "m0", "scc", ACLOB);
    asm volatile("s_nop 1\n\t"
        "v_mfma_f32_16x16x32_bf16 a[4:7], a[176:179], %[p1], a[4:7]\n\t"
        "v_exp_f32_e32 %[e30], %[s300]\n\t"
        "v_mfma_f32_16x16x32_bf16 a[20:23], a[180:183], %[p1], a[20:23]\n\t"
        "v_exp_f32_e32 %[e31], %[s301]\n\t"
        "v_mfma_f32_16x16x32_bf16 a[36:39], a[184:187], %[p1], a[36:39]\n\t"
        "v_exp_f32_e32 %[e32], %[s302]\n\t"
        "v_mfma_f32_16x16x32_bf16 a[52:55], a[188:191], %[p1], a[52:55]\n\t"
        "global_load_lds_dwordx4 %[dvo1], %[dsb] offset:1024\n\t"
        "v_mfma_f32_16x16x32_bf16 a[68:71], a[192:195], %[p1], a[68:71]\n\t"
        "v_exp_f32_e32 %[e33], %[s303]\n\t"
        "v_mfma_f32_16x16x32_bf16 a[84:87], a[196:199], %[p1], a[84:87]\n\t"
        "v_exp_f32_e32 %[e34], %[s310]\n\t"
        "v_mfma_f32_16x16x32_bf16 a[100:103], a[200:203], %[p1], a[100:103]\n\t"
        "v_exp_f32_e32 %[e35], %[s311]\n\t"
        "v_mfma_f32_16x16x32_bf16 a[116:119], a[204:207], %[p1], a[116:119]\n\t"
        "v_exp_f32_e32 %[e36], %[s312]\n\t"
        "v_mfma_f32_16x16x32_bf16 a[132:135], a[208:211], %[p1], a[132:135]\n\t"
        "v_exp_f32_e32 %[e37], %[s313]"
        : [e30] "=&v"(E[3][0]), [e31] "=&v"(E[3][1]), [e32] "=&v"(E[3][2]), [e33] "=&v"(E[3][3]), [e34] "=&v"(E[3][4]), [e35] "=&v"(E[3][5]), [e36] "=&v"(E[3][6]), [e37] "=&v"(E[3][7])
        : [p1] "v"(mk8(PW[1][0], PW[1][1], PW[1][2], PW[1][3])), [s300] "v"(S[3][0][0]), [s301] "v"(S[3][0][1]), [s302] "v"(S[3][0][2]), [dvo1] "v"(c.dvo[1]), [dsb] "s"(c.dsb[0]), [s303] "v"(S[3][0][3]), [s310] "v"(S[3][1][0]), [s311] "v"(S[3][1][1]), [s312] "v"(S[3][1][2]), [s313] "v"(S[3][1][3])
        : "memory", "m0", "scc", ACLOB);
    asm volatile("s_nop 1\n\t"
        "v_mfma_f32_16x16x32_bf16 a[8:11], a[176:179], %[p2], a[8:11]\n\t"
        "v_cvt_pk_bf16_f32 %[pw30], %[e30], %[e31]\n\t"
        "v_cvt_pk_bf16_f32 %[pw31], %[e32], %[e33]\n\t"
        "v_mfma_f32_16x16x32_bf16 a[24:27], a[180:183], %[p2], a[24:27]\n\t"
        "v_cvt_pk_bf16_f32 %[pw32], %[e34], %[e35]\n\t"
        "v_cvt_pk_bf16_f32 %[pw33], %[e36], %[e37]\n\t"
        "v_mfma_f32_16x16x32_bf16 a[40:43], a[184:187], %[p2], a[40:43]\n\t"
        "ds_read_b128 a[144:147], %[ka] offset:8192\n\t"
        "ds_read_b128 a[148:151], %[ka] offset:9216\n\t"
        "v_mfma_f32_16x16x32_bf16 a[56:59], a[188:191], %[p2], a[56:59]\n\t"
        "global_load_lds_dwordx4 %[dvo1], %[dsb] offset:2048\n\t"
        "v_mfma_f32_16x16x32_bf16 a[72:75], a[192:195], %[p2], a[72:75]\n\t"
        "ds_read_b128 a[152:155], %[ka] offset:10240\n\t"
        "ds_read_b128 a[156:159], %[ka] offset:11264\n\t"
        "v_mfma_f32_16x16x32_bf16 a[88:91], a[196:199], %[p2], a[88:91]\n\t"
        "ds_read_b128 a[160:163], %[ka] offset:12288\n\t"
        "ds_read_b128 a[164:167], %[ka] offset:13312\n\t"
        "v_mfma_f32_16x16x32_bf16 a[104:107], a[200:203], %[p2], a[104:107]\n\t"
        "ds_read_b128 a[168:171], %[ka] offset:14336\n\t"
        "ds_read_b128 a[172:175], %[ka] offset:15360\n\t"
        "v_mfma_f32_16x16x32_bf16 a[120:123], a[204:207], %[p2], a[120:123]\n\t"
        "v_mfma_f32_16x16x32_bf16 a[136:139], a[208:211], %[p2], a[136:139]"
        : [pw30] "=&v"(PW[3][0]), [pw31] "=&v"(PW[3][1]), [pw32] "=&v"(PW[3][2]), [pw33] "=&v"(PW[3][3])
        : [p2] "v"(mk8(PW[2][0], PW[2][1], PW[2][2], PW[2][3])), [e30] "v"(E[3][0]), [e31] "v"(E[3][1]), [e32] "v"(E[3][2]), [e33] "v"(E[3][3]), [e34] "v"(E[3][4]), [e35] "v"(E[3][5]), [e36] "v"(E[3][6]), [e37] "v"(E[3][7]), [ka] "v"(c.ka), [dvo1] "v"(c.dvo[1]), [dsb] "s"(c.dsb[0])
        : "memory", "m0", "scc", ACLOB);
    asm volatile("s_nop 1\n\t"
        "v_mfma_f32_16x16x32_bf16 a[12:15], a[176:179], %[p3], a[12:15]\n\t"
        "v_mfma_f32_16x16x32_bf16 a[28:31], a[180:183], %[p3], a[28:31]\n\t"
        "v_mfma_f32_16x16x32_bf16 a[44:47], a[184:187], %[p3], a[44:47]\n\t"
        "v_mfma_f32_16x16x32_bf16 a[60:63], a[188:191], %[p3], a[60:63]\n\t"
        "global_load_lds_dwordx4 %[dvo1], %[dsb] offset:3072\n\t"
        "global_load_dwordx4 a[252:255], %[dvo0], %[pfs0]\n\t"
        "v_mfma_f32_16x16x32_bf16 a[76:79], a[192:195], %[p3], a[76:79]\n\t"
        "v_mfma_f32_16x16x32_bf16 a[92:95], a[196:199], %[p3], a[92:95]\n\t"
        "v_mfma_f32_16x16x32_bf16 a[108:111], a[200:203], %[p3], a[108:111]\n\t"
        "v_mfma_f32_16x16x32_bf16 a[124:127], a[204:207], %[p3], a[124:127]\n\t"
        "v_mfma_f32_16x16x32_bf16 a[140:143], a[208:211], %[p3], a[140:143]"
        :
        : [p3] "v"(mk8(PW[3][0], PW[3][1], PW[3][2], PW[3][3])), [dvo1] "v"(c.dvo[1]), [dsb] "s"(c.dsb[0]), [dvo0] "v"(c.dvo[0]), [pfs0] "s"(c.pfs[0])
        : "memory", "m0", "scc", ACLOB);
}
DI void att_half_0s(AttC& c) {
    f32x4 S[4][2]; float E[4][8]; unsigned PW[4][4];
    asm volatile("s_waitcnt vmcnt(10) lgkmcnt(0)\n\t"
        "s_barrier\n\t"
        "s_add_u32 m0, %[dm], 0\n\t"
        "s_nop 0\n\t"
        "global_load_lds_dwordx4 %[dvo0], %[dsb] offset:0"
        :
        : [dm] "s"(c.dm0[0]), [dvo0] "v"(c.dvo[0]), [dsb] "s"(c.dsb[0])
        : "memory", "m0", "scc", ACLOB);
    asm volatile("global_load_lds_dwordx4 %[dvo0], %[dsb] offset:1024"
        :
        : [dvo0] "v"(c.dvo[0]), [dsb] "s"(c.dsb[0])
        : "memory", "m0", "scc", ACLOB);
    asm volatile("global_load_lds_dwordx4 %[dvo0], %[dsb] offset:2048"
        :
        : [dvo0] "v"(c.dvo[0]), [dsb] "s"(c.dsb[0])
        : "memory", "m0", "scc", ACLOB);
    asm volatile("global_load_lds_dwordx4 %[dvo0], %[dsb] offset:3072"
        :
        : [dvo0] "v"(c.dvo[0]), [dsb] "s"(c.dsb[0])
        : "memory", "m0", "scc", ACLOB);
    asm volatile("s_waitcnt lgkmcnt(0)\n\t"
        "s_add_u32 m0, %[dm], 4096\n\t"
        "s_nop 0\n\t"
        "global_load_lds_dwordx4 %[dvo1], %[dsb] offset:0"
        :
        : [dm] "s"(c.dm0[0]), [dvo1] "v"(c.dvo[1]), [dsb] "s"(c.dsb[0])
        : "memory", "m0", "scc", ACLOB);
    asm volatile("ds_read_b128 a[144:147], %[ka] offset:8192\n\t"
        "s_nop 0\n\t"
        "ds_read_b128 a[148:151], %[ka] offset:9216\n\t"
        "s_nop 0\n\t"
        "ds_read_b128 a[152:155], %[ka] offset:10240\n\t"
        "s_nop 0\n\t"
        "ds_read_b128 a[156:159], %[ka] offset:11264\n\t"
        "s_nop 0\n\t"
        "ds_read_b128 a[160:163], %[ka] offset:12288\n\t"
        "s_nop 0\n\t"
        "ds_read_b128 a[164:167], %[ka] offset:13312\n\t"
        "s_nop 0\n\t"
        "global_load_lds_dwordx4 %[dvo1], %[dsb] offset:1024"
        :
        : [ka] "v"(c.ka), [dvo1] "v"(c.dvo[1]), [dsb] "s"(c.dsb[0])
        : "memory", "m0", "scc", ACLOB);
    asm volatile("ds_read_b128 a[168:171], %[ka] offset:14336\n\t"
        "s_nop 0\n\t"
        "ds_read_b128 a[172:175], %[ka] offset:15360\n\t"
        "s_nop 0\n\t"
        "global_load_lds_dwordx4 %[dvo1], %[dsb] offset:2048"
        :
        : [ka] "v"(c.ka), [dvo1] "v"(c.dvo[1]), [dsb] "s"(c.dsb[0])
        : "memory", "m0", "scc", ACLOB);
    asm volatile("global_load_lds_dwordx4 %[dvo1], %[dsb] offset:3072\n\t"
        "global_load_dwordx4 a[252:255], %[dvo0], %[pfs0]"
        :
        : [dvo1] "v"(c.dvo[1]), [dsb] "s"(c.dsb[0]), [dvo0] "v"(c.dvo[0]), [pfs0] "s"(c.pfs[0])
        : "memory", "m0", "scc", ACLOB);
}
DI void att_half_1n(AttC& c) {
    f32x4 S[4][2]; float E[4][8]; unsigned PW[4][4];
    asm volatile("s_waitcnt vmcnt(10) lgkmcnt(0)\n\t"
        "s_barrier\n\t"
        "v_mfma_f32_16x16x32_bf16 %[S00], a[144:147], %[q00], %[negm0]\n\t"
        "ds_read_b64_tr_b16 a[176:177], %[vb0] offset:8192\n\t"
        "ds_read_b64_tr_b16 a[178:179], %[vb0] offset:12288\n\t"
        "v_mfma_f32_16x16x32_bf16 %[S01], a[160:163], %[q00], %[negm0]\n\t"
        "ds_read_b64_tr_b16 a[180:181], %[vb1] offset:8192\n\t"
        "ds_read_b64_tr_b16 a[182:183], %[vb1] offset:12288\n\t"
        "v_mfma_f32_16x16x32_bf16 %[S00], a[148:151], %[q01], %[S00]\n\t"
        "ds_read_b64_tr_b16 a[184:185], %[vb0] offset:9216\n\t"
        "ds_read_b64_tr_b16 a[186:187], %[vb0] offset:13312\n\t"
        "v_mfma_f32_16x16x32_bf16 %[S01], a[164:167], %[q01], %[S01]\n\t"
        "s_add_u32 m0, %[dm], 0\n\t"
        "s_nop 0\n\t"
        "global_load_lds_dwordx4 %[dvo0], %[dsb] offset:0\n\t"
        "v_mfma_f32_16x16x32_bf16 %[S00], a[152:155], %[q02], %[S00]\n\t"
        "ds_read_b64_tr_b16 a[188:189], %[vb1] offset:9216\n\t"
        "ds_read_b64_tr_b16 a[190:191], %[vb1] offset:13312\n\t"
        "v_mfma_f32_16x16x32_bf16 %[S01], a[168:171], %[q02], %[S01]\n\t"
        "ds_read_b64_tr_b16 a[192:193], %[vb0] offset:10240\n\t"
        "ds_read_b64_tr_b16 a[194:195], %[vb0] offset:14336\n\t"
        "v_mfma_f32_16x16x32_bf16 %[S00], a[156:159], %[q03], %[S00]\n\t"
        "ds_read_b64_tr_b16 a[196:197], %[vb1] offset:10240\n\t"
        "ds_read_b64_tr_b16 a[198:199], %[vb1] offset:14336\n\t"
        "v_mfma_f32_16x16x32_bf16 %[S01], a[172:175], %[q03], %[S01]\n\t"
        "ds_read_b64_tr_b16 a[200:201], %[vb0] offset:11264\n\t"
        "ds_read_b64_tr_b16 a[202:203], %[vb0] offset:15360"
        : [S00] "=&v"(S[0][0]), [S01] "=&v"(S[0][1])
        : [q00] "v"(c.qf[0][0]), [negm0] "v"(c.negm[0]), [vb0] "v"(c.vb[0]), [vb1] "v"(c.vb[1]), [q01] "v"(c.qf[0][1]), [dm] "s"(c.dm0[1]), [dvo0] "v"(c.dvo[0]), [dsb] "s"(c.dsb[1]), [q02] "v"(c.qf[0][2]), [q03] "v"(c.qf[0][3])
        : "memory", "m0", "scc", ACLOB);
    asm volatile("v_mfma_f32_16x16x32_bf16 %[S10], a[144:147], %[q10], %[negm1]\n\t"
        "ds_read_b64_tr_b16 a[204:205], %[vb1] offset:11264\n\t"
        "ds_read_b64_tr_b16 a[206:207], %[vb1] offset:15360\n\t"
        "v_mfma_f32_16x16x32_bf16 %[S11], a[160:163], %[q10], %[negm1]\n\t"
        "v_mfma_f32_16x16x32_bf16 %[S10], a[148:151], %[q11], %[S10]\n\t"
        "v_exp_f32_e32 %[e00], %[s000]\n\t"
        "v_mfma_f32_16x16x32_bf16 %[S11], a[164:167], %[q11], %[S11]\n\t"
        "global_load_lds_dwordx4 %[dvo0], %[dsb] offset:1024\n\t"
        "v_mfma_f32_16x16x32_bf16 %[S10], a[152:155], %[q12], %[S10]\n\t"
        "v_exp_f32_e32 %[e01], %[s001]\n\t"
        "v_mfma_f32_16x16x32_bf16 %[S11], a[168:171], %[q12], %[S11]\n\t"
        "v_exp_f32_e32 %[e02], %[s002]\n\t"
        "v_mfma_f32_16x16x32_bf16 %[S10], a[156:159], %[q13], %[S10]\n\t"
        "v_exp_f32_e32 %[e03], %[s003]\n\t"
        "v_mfma_f32_16x16x32_bf16 %[S11], a[172:175], %[q13], %[S11]\n\t"
        "v_exp_f32_e32 %[e04], %[s010]"
        : [S10] "=&v"(S[1][0]), [S11] "=&v"(S[1][1]), [e00] "=&v"(E[0][0]), [e01] "=&v"(E[0][1]), [e02] "=&v"(E[0][2]), [e03] "=&v"(E[0][3]), [e04] "=&v"(E[0][4])
        : [q10] "v"(c.qf[1][0]), [negm1] "v"(c.negm[1]), [vb1] "v"(c.vb[1]), [q11] "v"(c.qf[1][1]), [s000] "v"(S[0][0][0]), [dvo0] "v"(c.dvo[0]), [dsb] "s"(c.dsb[1]), [q12] "v"(c.qf[1][2]), [s001] "v"(S[0][0][1]), [s002] "v"(S[0][0][2]), [q13] "v"(c.qf[1][3]), [s003] "v"(S[0][0][3]), [s010] "v"(S[0][1][0])
        : "memory", "m0", "scc", ACLOB);
    asm volatile("v_mfma_f32_16x16x32_bf16 %[S20], a[144:147], %[q20], %[negm2]\n\t"
        "v_exp_f32_e32 %[e05], %[s011]\n\t"
        "v_mfma_f32_16x16x32_bf16 %[S21], a[160:163], %[q20], %[negm2]\n\t"
        "v_exp_f32_e32 %[e06], %[s012]\n\t"
        "v_mfma_f32_16x16x32_bf16 %[S20], a[148:151], %[q21], %[S20]\n\t"
        "v_exp_f32_e32 %[e07], %[s013]\n\t"
        "v_mfma_f32_16x16x32_bf16 %[S21], a[164:167], %[q21], %[S21]\n\t"
        "global_load_lds_dwordx4 %[dvo0], %[dsb] offset:2048\n\t"
        "v_mfma_f32_16x16x32_bf16 %[S20], a[152:155], %[q22], %[S20]\n\t"
        "v_cvt_pk_bf16_f32 %[pw00], %[e00], %[e01]\n\t"
        "v_cvt_pk_bf16_f32 %[pw01], %[e02], %[e03]\n\t"
        "v_mfma_f32_16x16x32_bf16 %[S21], a[168:171], %[q22], %[S21]\n\t"
        "v_cvt_pk_bf16_f32 %[pw02], %[e04], %[e05]\n\t"
        "v_cvt_pk_bf16_f32 %[pw03], %[e06], %[e07]\n\t"
        "v_mfma_f32_16x16x32_bf16 %[S20], a[156:159], %[q23], %[S20]\n\t"
        "v_exp_f32_e32 %[e10], %[s100]\n\t"
        "v_mfma_f32_16x16x32_bf16 %[S21], a[172:175], %[q23], %[S21]\n\t"
        "v_exp_f32_e32 %[e11], %[s101]"
        : [S20] "=&v"(S[2][0]), [e05] "=&v"(E[0][5]), [S21] "=&v"(S[2][1]), [e06] "=&v"(E[0][6]), [e07] "=&v"(E[0][7]), [pw00] "=&v"(PW[0][0]), [pw01] "=&v"(PW[0][1]), [pw02] "=&v"(PW[0][2]), [pw03] "=&v"(PW[0][3]), [e10] "=&v"(E[1][0]), [e11] "=&v"(E[1][1])
        : [q20] "v"(c.qf[2][0]), [negm2] "v"(c.negm[2]), [s011] "v"(S[0][1][1]), [s012] "v"(S[0][1][2]), [q21] "v"(c.qf[2][1]), [s013] "v"(S[0][1][3]), [dvo0] "v"(c.dvo[0]), [dsb] "s"(c.dsb[1]), [q22] "v"(c.qf[2][2]), [e00] "v"(E[0][0]), [e01] "v"(E[0][1]), [e02] "v"(E[0][2]), [e03] "v"(E[0][3]), [e04] "v"(E[0][4]), [q23] "v"(c.qf[2][3]), [s100] "v"(S[1][0][0]), [s101] "v"(S[1][0][1])
        : "memory", "m0", "scc", ACLOB);
    asm volatile("v_mfma_f32_16x16x32_bf16 %[S30], a[144:147], %[q30], %[negm3]\n\t"
        "v_exp_f32_e32 %[e12], %[s102]\n\t"
        "v_mfma_f32_16x16x32_bf16 %[S31], a[160:163], %[q30], %[negm3]\n\t"
        "v_exp_f32_e32 %[e13], %[s103]\n\t"
        "v_mfma_f32_16x16x32_bf16 %[S30], a[148:151], %[q31], %[S30]\n\t"
        "v_exp_f32_e32 %[e14], %[s110]\n\t"
        "v_mfma_f32_16x16x32_bf16 %[S31], a[164:167], %[q31], %[S31]\n\t"
        "global_load_lds_dwordx4 %[dvo0], %[dsb] offset:3072\n\t"
        "v_mfma_f32_16x16x32_bf16 %[S30], a[152:155], %[q32], %[S30]\n\t"
        "v_exp_f32_e32 %[e15], %[s111]\n\t"
        "v_mfma_f32_16x16x32_bf16 %[S31], a[168:171], %[q32], %[S31]\n\t"
        "v_exp_f32_e32 %[e16], %[s112]\n\t"
        "v_mfma_f32_16x16x32_bf16 %[S30], a[156:159], %[q33], %[S30]\n\t"
        "v_exp_f32_e32 %[e17], %[s113]\n\t"
        "v_mfma_f32_16x16x32_bf16 %[S31], a[172:175], %[q33], %[S31]\n\t"
        "v_cvt_pk_bf16_f32 %[pw10], %[e10], %[e11]\n\t"
        "v_cvt_pk_bf16_f32 %[pw11], %[e12], %[e13]"
        : [S30] "=&v"(S[3][0]), [e12] "=&v"(E[1][2]), [S31] "=&v"(S[3][1]), [e13] "=&v"(E[1][3]), [e14] "=&v"(E[1][4]), [e15] "=&v"(E[1][5]), [e16] "=&v"(E[1][6]), [e17] "=&v"(E[1][7]), [pw10] "=&v"(PW[1][0]), [pw11] "=&v"(PW[1][1])
        : [q30] "v"(c.qf[3][0]), [negm3] "v"(c.negm[3]), [s102] "v"(S[1][0][2]), [s103] "v"(S[1][0][3]), [q31] "v"(c.qf[3][1]), [s110] "v"(S[1][1][0]), [dvo0] "v"(c.dvo[0]), [dsb] "s"(c.dsb[1]), [q32] "v"(c.qf[3][2]), [s111] "v"(S[1][1][1]), [s112] "v"(S[1][1][2]), [q33] "v"(c.qf[3][3]), [s113] "v"(S[1][1][3]), [e10] "v"(E[1][0]), [e11] "v"(E[1][1])
        : "memory", "m0", "scc", ACLOB);
    asm volatile("s_waitcnt lgkmcnt(0)\n\t"
        "s_nop 1\n\t"
        "v_mfma_f32_16x16x32_bf16 a[0:3], a[176:179], %[p0], a[0:3]\n\t"
        "v_cvt_pk_bf16_f32 %[pw12], %[e14], %[e15]\n\t"
        "v_cvt_pk_bf16_f32 %[pw13], %[e16], %[e17]\n\t"
        "v_mfma_f32_16x16x32_bf16 a[16:19], a[180:183], %[p0], a[16:19]\n\t"
        "v_exp_f32_e32 %[e20], %[s200]\n\t"
        "v_mfma_f32_16x16x32_bf16 a[32:35], a[184:187], %[p0], a[32:35]\n\t"
        "v_exp_f32_e32 %[e21], %[s201]\n\t"
        "v_mfma_f32_16x16x32_bf16 a[48:51], a[188:191], %[p0], a[48:51]\n\t"
        "s_add_u32 m0, %[dm], 4096\n\t"
        "s_nop 0\n\t"
        "global_load_lds_dwordx4 %[dvo1], %[dsb] offset:0\n\t"
        "v_mfma_f32_16x16x32_bf16 a[64:67], a[192:195], %[p0], a[64:67]\n\t"
        "v_exp_f32_e32 %[e22], %[s202]\n\t"
        "v_mfma_f32_16x16x32_bf16 a[80:83], a[196:199], %[p0], a[80:83]\n\t"
        "v_exp_f32_e32 %[e23], %[s203]\n\t"
        "v_mfma_f32_16x16x32_bf16 a[96:99], a[200:203], %[p0], a[96:99]\n\t"
        "v_exp_f32_e32 %[e24], %[s210]\n\t"
        "v_mfma_f32_16x16x32_bf16 a[112:115], a[204:207], %[p0], a[112:115]\n\t"
        "v_exp_f32_e32 %[e25], %[s211]\n\t"
        "v_mfma_f32_16x16x32_bf16 a[128:131], a[208:211], %[p0], a[128:131]\n\t"
        "v_exp_f32_e32 %[e26], %[s212]"
        : [pw12] "=&v"(PW[1][2]), [pw13] "=&v"(PW[1][3]), [e20] "=&v"(E[2][0]), [e21] "=&v"(E[2][1]), [e22] "=&v"(E[2][2]), [e23] "=&v"(E[2][3]), [e24] "=&v"(E[2][4]), [e25] "=&v"(E[2][5]), [e26] "=&v"(E[2][6])
        : [p0] "v"(mk8(PW[0][0], PW[0][1], PW[0][2], PW[0][3])), [e14] "v"(E[1][4]), [e15] "v"(E[1][5]), [e16] "v"(E[1][6]), [e17] "v"(E[1][7]), [s200] "v"(S[2][0][0]), [s201] "v"(S[2][0][1]), [dm] "s"(c.dm0[1]), [dvo1] "v"(c.dvo[1]), [dsb] "s"(c.dsb[1]), [s202] "v"(S[2][0][2]), [s203] "v"(S[2][0][3]), [s210] "v"(S[2][1][0]), [s211] "v"(S[2][1][1]), [s212] "v"(S[2][1][2])
        : "memory", "m0", "scc", ACLOB);
    asm volatile("s_nop 1\n\t"
        "v_mfma_f32_16x16x32_bf16 a[4:7], a[176:179], %[p1], a[4:7]\n\t"
        "v_exp_f32_e32 %[e27], %[s213]\n\t"
        "v_mfma_f32_16x16x32_bf16 a[20:23], a[180:183], %[p1], a[20:23]\n\t"
        "v_cvt_pk_bf16_f32 %[pw20], %[e20], %[e21]\n\t"
        "v_cvt_pk_bf16_f32 %[pw21], %[e22], %[e23]\n\t"
        "v_mfma_f32_16x16x32_bf16 a[36:39], a[184:187], %[p1], a[36:39]\n\t"
        "v_cvt_pk_bf16_f32 %[pw22], %[e24], %[e25]\n\t"
        "v_cvt_pk_bf16_f32 %[pw23], %[e26], %[e27]\n\t"
        "v_mfma_f32_16x16x32_bf16 a[52:55], a[188:191], %[p1], a[52:55]\n\t"
        "global_load_lds_dwordx4 %[dvo1], %[dsb] offset:1024\n\t"
        "v_mfma_f32_16x16x32_bf16 a[68:71], a[192:195], %[p1], a[68:71]\n\t"
        "v_exp_f32_e32 %[e30], %[s300]\n\t"
        "v_mfma_f32_16x16x32_bf16 a[84:87], a[196:199], %[p1], a[84:87]\n\t"
        "v_exp_f32_e32 %[e31], %[s301]\n\t"
        "v_mfma_f32_16x16x32_bf16 a[100:103], a[200:203], %[p1], a[100:103]\n\t"
        "v_exp_f32_e32 %[e32], %[s302]\n\t"
        "v_mfma_f32_16x16x32_bf16 a[116:119], a[204:207], %[p1], a[116:119]\n\t"
        "v_exp_f32_e32 %[e33], %[s303]\n\t"
        "v_mfma_f32_16x16x32_bf16 a[132:135], a[208:211], %[p1], a[132:135]\n\t"
        "v_exp_f32_e32 %[e34], %[s310]"
        : [e27] "=&v"(E[2][7]), [pw20] "=&v"(PW[2][0]), [pw21] "=&v"(PW[2][1]), [pw22] "=&v"(PW[2][2]), [pw23] "=&v"(PW[2][3]), [e30] "=&v"(E[3][0]), [e31] "=&v"(E[3][1]), [e32] "=&v"(E[3][2]), [e33] "=&v"(E[3][3]), [e34] "=&v"(E[3][4])
        : [p1] "v"(mk8(PW[1][0], PW[1][1], PW[1][2], PW[1][3])), [s213] "v"(S[2][1][3]), [e20] "v"(E[2][0]), [e21] "v"(E[2][1]), [e22] "v"(E[2][2]), [e23] "v"(E[2][3]), [e24] "v"(E[2][4]), [e25] "v"(E[2][5]), [e26] "v"(E[2][6]), [dvo1] "v"(c.dvo[1]), [dsb] "s"(c.dsb[1]), [s300] "v"(S[3][0][0]), [s301] "v"(S[3][0][1]), [s302] "v"(S[3][0][2]), [s303] "v"(S[3][0][3]), [s310] "v"(S[3][1][0])
        : "memory", "m0", "scc", ACLOB);
    asm volatile("s_nop 1\n\t"
        "v_mfma_f32_16x16x32_bf16 a[8:11], a[176:179], %[p2], a[8:11]\n\t"
        "v_exp_f32_e32 %[e35], %[s311]\n\t"
        "v_mfma_f32_16x16x32_bf16 a[24:27], a[180:183], %[p2], a[24:27]\n\t"
        "v_exp_f32_e32 %[e36], %[s312]\n\t"
        "v_mfma_f32_16x16x32_bf16 a[40:43], a[184:187], %[p2], a[40:43]\n\t"
        "v_exp_f32_e32 %[e37], %[s313]\n\t"
        "v_mfma_f32_16x16x32_bf16 a[56:59], a[188:191], %[p2], a[56:59]\n\t"
        "global_load_lds_dwordx4 %[dvo1], %[dsb] offset:2048\n\t"
        "v_mfma_f32_16x16x32_bf16 a[72:75], a[192:195], %[p2], a[72:75]\n\t"
        "v_cvt_pk_bf16_f32 %[pw30], %[e30], %[e31]\n\t"
        "v_cvt_pk_bf16_f32 %[pw31], %[e32], %[e33]\n\t"
        "v_mfma_f32_16x16x32_bf16 a[88:91], a[196:199], %[p2], a[88:91]\n\t"
        "v_cvt_pk_bf16_f32 %[pw32], %[e34], %[e35]\n\t"
        "v_cvt_pk_bf16_f32 %[pw33], %[e36], %[e37]\n\t"
        "v_mfma_f32_16x16x32_bf16 a[104:107], a[200:203], %[p2], a[104:107]\n\t"
        "ds_read_b128 a[144:147], %[kan] offset:0\n\t"
        "ds_read_b128 a[148:151], %[kan] offset:1024\n\t"
        "v_mfma_f32_16x16x32_bf16 a[120:123], a[204:207], %[p2], a[120:123]\n\t"
        "ds_read_b128 a[152:155], %[kan] offset:2048\n\t"
        "ds_read_b128 a[156:159], %[kan] offset:3072\n\t"
        "v_mfma_f32_16x16x32_bf16 a[136:139], a[208:211], %[p2], a[136:139]\n\t"
        "ds_read_b128 a[160:163], %[kan] offset:4096\n\t"
        "ds_read_b128 a[164:167], %[kan] offset:5120"
        : [e35] "=&v"(E[3][5]), [e36] "=&v"(E[3][6]), [e37] "=&v"(E[3][7]), [pw30] "=&v"(PW[3][0]), [pw31] "=&v"(PW[3][1]), [pw32] "=&v"(PW[3][2]), [pw33] "=&v"(PW[3][3])
        : [p2] "v"(mk8(PW[2][0], PW[2][1], PW[2][2], PW[2][3])), [s311] "v"(S[3][1][1]), [s312] "v"(S[3][1][2]), [s313] "v"(S[3][1][3]), [dvo1] "v"(c.dvo[1]), [dsb] "s"(c.dsb[1]), [e30] "v"(E[3][0]), [e31] "v"(E[3][1]), [e32] "v"(E[3][2]), [e33] "v"(E[3][3]), [e34] "v"(E[3][4]), [kan] "v"(c.kan)
        : "memory", "m0", "scc", ACLOB);
    asm volatile("s_nop 1\n\t"
        "v_mfma_f32_16x16x32_bf16 a[12:15], a[176:179], %[p3], a[12:15]\n\t"
        "ds_read_b128 a[168:171], %[kan] offset:6144\n\t"
        "ds_read_b128 a[172:175], %[kan] offset:7168\n\t"
        "v_mfma_f32_16x16x32_bf16 a[28:31], a[180:183], %[p3], a[28:31]\n\t"
        "v_mfma_f32_16x16x32_bf16 a[44:47], a[184:187], %[p3], a[44:47]\n\t"
        "v_mfma_f32_16x16x32_bf16 a[60:63], a[188:191], %[p3], a[60:63]\n\t"
        "global_load_lds_dwordx4 %[dvo1], %[dsb] offset:3072\n\t"
        "global_load_dwordx4 a[252:255], %[dvo0], %[pfs1]\n\t"
        "v_mfma_f32_16x16x32_bf16 a[76:79], a[192:195], %[p3], a[76:79]\n\t"
        "v_mfma_f32_16x16x32_bf16 a[92:95], a[196:199], %[p3], a[92:95]\n\t"
        "v_mfma_f32_16x16x32_bf16 a[108:111], a[200:203], %[p3], a[108:111]\n\t"
        "v_mfma_f32_16x16x32_bf16 a[124:127], a[204:207], %[p3], a[124:127]\n\t"
        "v_mfma_f32_16x16x32_bf16 a[140:143], a[208:211], %[p3], a[140:143]"
        :
        : [p3] "v"(mk8(PW[3][0], PW[3][1], PW[3][2], PW[3][3])), [kan] "v"(c.kan), [dvo1] "v"(c.dvo[1]), [dsb] "s"(c.dsb[1]), [dvo0] "v"(c.dvo[0]), [pfs1] "s"(c.pfs[1])
        : "memory", "m0", "scc", ACLOB);
}
DI void att_half_1nx(AttC& c) {
    f32x4 S[4][2]; float E[4][8]; unsigned PW[4][4];
    asm volatile("s_waitcnt vmcnt(10) lgkmcnt(0)\n\t"
        "s_barrier\n\t"
        "v_mfma_f32_16x16x32_bf16 %[S00], a[144:147], %[q00], %[negm0]\n\t"
        "ds_read_b64_tr_b16 a[176:177], %[vb0] offset:8192\n\t"
        "ds_read_b64_tr_b16 a[178:179], %[vb0] offset:12288\n\t"
        "v_mfma_f32_16x16x32_bf16 %[S01], a[160:163], %[q00], %[negm0]\n\t"
        "ds_read_b64_tr_b16 a[180:181], %[vb1] offset:8192\n\t"
        "ds_read_b64_tr_b16 a[182:183], %[vb1] offset:12288\n\t"
        "v_mfma_f32_16x16x32_bf16 %[S00], a[148:151], %[q01], %[S00]\n\t"
        "ds_read_b64_tr_b16 a[184:185], %[vb0] offset:9216\n\t"
        "ds_read_b64_tr_b16 a[186:187], %[vb0] offset:13312\n\t"
        "v_mfma_f32_16x16x32_bf16 %[S01], a[164:167], %[q01], %[S01]\n\t"
        "ds_read_b64_tr_b16 a[188:189], %[vb1] offset:9216\n\t"
        "ds_read_b64_tr_b16 a[190:191], %[vb1] offset:13312\n\t"
        "v_mfma_f32_16x16x32_bf16 %[S00], a[152:155], %[q02], %[S00]\n\t"
        "ds_read_b64_tr_b16 a[192:193], %[vb0] offset:10240\n\t"
        "ds_read_b64_tr_b16 a[194:195], %[vb0] offset:14336\n\t"
        "v_mfma_f32_16x16x32_bf16 %[S01], a[168:171], %[q02], %[S01]\n\t"
        "ds_read_b64_tr_b16 a[196:197], %[vb1] offset:10240\n\t"
        "ds_read_b64_tr_b16 a[198:199], %[vb1] offset:14336\n\t"
        "v_mfma_f32_16x16x32_bf16 %[S00], a[156:159], %[q03], %[S00]\n\t"
        "ds_read_b64_tr_b16 a[200:201], %[vb0] offset:11264\n\t"
        "ds_read_b64_tr_b16 a[202:203], %[vb0] offset:15360\n\t"
        "v_mfma_f32_16x16x32_bf16 %[S01], a[172:175], %[q03], %[S01]\n\t"
        "ds_read_b64_tr_b16 a[204:205], %[vb1] offset:11264\n\t"
        "ds_read_b64_tr_b16 a[206:207], %[vb1] offset:15360"
        : [S00] "=&v"(S[0][0]), [S01] "=&v"(S[0][1])
        : [q00] "v"(c.qf[0][0]), [negm0] "v"(c.negm[0]), [vb0] "v"(c.vb[0]), [vb1] "v"(c.vb[1]), [q01] "v"(c.qf[0][1]), [q02] "v"(c.qf[0][2]), [q03] "v"(c.qf[0][3])
        : "memory", "m0", "scc", ACLOB);
    asm volatile("v_mfma_f32_16x16x32_bf16 %[S10], a[144:147], %[q10], %[negm1]\n\t"
        "v_mfma_f32_16x16x32_bf16 %[S11], a[160:163], %[q10], %[negm1]\n\t"
        "v_mfma_f32_16x16x32_bf16 %[S10], a[148:151], %[q11], %[S10]\n\t"
        "v_exp_f32_e32 %[e00], %[s000]\n\t"
        "v_mfma_f32_16x16x32_bf16 %[S11], a[164:167], %[q11], %[S11]\n\t"
        "v_exp_f32_e32 %[e01], %[s001]\n\t"
        "v_mfma_f32_16x16x32_bf16 %[S10], a[152:155], %[q12], %[S10]\n\t"
        "v_exp_f32_e32 %[e02], %[s002]\n\t"
        "v_mfma_f32_16x16x32_bf16 %[S11], a[168:171], %[q12], %[S11]\n\t"
        "v_exp_f32_e32 %[e03], %[s003]\n\t"
        "v_mfma_f32_16x16x32_bf16 %[S10], a[156:159], %[q13], %[S10]\n\t"
        "v_exp_f32_e32 %[e04], %[s010]\n\t"
        "v_mfma_f32_16x16x32_bf16 %[S11], a[172:175], %[q13], %[S11]\n\t"
        "v_exp_f32_e32 %[e05], %[s011]"
        : [S10] "=&v"(S[1][0]), [S11] "=&v"(S[1][1]), [e00] "=&v"(E[0][0]), [e01] "=&v"(E[0][1]), [e02] "=&v"(E[0][2]), [e03] "=&v"(E[0][3]), [e04] "=&v"(E[0][4]), [e05] "=&v"(E[0][5])
        : [q10] "v"(c.qf[1][0]), [negm1] "v"(c.negm[1]), [q11] "v"(c.qf[1][1]), [s000] "v"(S[0][0][0]), [s001] "v"(S[0][0][1]), [q12] "v"(c.qf[1][2]), [s002] "v"(S[0][0][2]), [s003] "v"(S[0][0][3]), [q13] "v"(c.qf[1][3]), [s010] "v"(S[0][1][0]), [s011] "v"(S[0][1][1])
        : "memory", "m0", "scc", ACLOB);
    asm volatile("v_mfma_f32_16x16x32_bf16 %[S20], a[144:147], %[q20], %[negm2]\n\t"
        "v_exp_f32_e32 %[e06], %[s012]\n\t"
        "v_mfma_f32_16x16x32_bf16 %[S21], a[160:163], %[q20], %[negm2]\n\t"
        "v_exp_f32_e32 %[e07], %[s013]\n\t"
        "v_mfma_f32_16x16x32_bf16 %[S20], a[148:151], %[q21], %[S20]\n\t"
        "v_cvt_pk_bf16_f32 %[pw00], %[e00], %[e01]\n\t"
        "v_cvt_pk_bf16_f32 %[pw01], %[e02], %[e03]\n\t"
        "v_mfma_f32_16x16x32_bf16 %[S21], a[164:167], %[q21], %[S21]\n\t"
        "v_cvt_pk_bf16_f32 %[pw02], %[e04], %[e05]\n\t"
        "v_cvt_pk_bf16_f32 %[pw03], %[e06], %[e07]\n\t"
        "v_mfma_f32_16x16x32_bf16 %[S20], a[152:155], %[q22], %[S20]\n\t"
        "v_exp_f32_e32 %[e10], %[s100]\n\t"
        "v_mfma_f32_16x16x32_bf16 %[S21], a[168:171], %[q22], %[S21]\n\t"
        "v_exp_f32_e32 %[e11], %[s101]\n\t"
        "v_mfma_f32_16x16x32_bf16 %[S20], a[156:159], %[q23], %[S20]\n\t"
        "v_exp_f32_e32 %[e12], %[s102]\n\t"
        "v_mfma_f32_16x16x32_bf16 %[S21], a[172:175], %[q23], %[S21]\n\t"
        "v_exp_f32_e32 %[e13], %[s103]"
        : [S20] "=&v"(S[2][0]), [e06] "=&v"(E[0][6]), [S21] "=&v"(S[2][1]), [e07] "=&v"(E[0][7]), [pw00] "=&v"(PW[0][0]), [pw01] "=&v"(PW[0][1]), [pw02] "=&v"(PW[0][2]), [pw03] "=&v"(PW[0][3]), [e10] "=&v"(E[1][0]), [e11] "=&v"(E[1][1]), [e12] "=&v"(E[1][2]), [e13] "=&v"(E[1][3])
        : [q20] "v"(c.qf[2][0]), [negm2] "v"(c.negm[2]), [s012] "v"(S[0][1][2]), [s013] "v"(S[0][1][3]), [q21] "v"(c.qf[2][1]), [e00] "v"(E[0][0]), [e01] "v"(E[0][1]), [e02] "v"(E[0][2]), [e03] "v"(E[0][3]), [e04] "v"(E[0][4]), [e05] "v"(E[0][5]), [q22] "v"(c.qf[2][2]), [s100] "v"(S[1][0][0]), [s101] "v"(S[1][0][1]), [q23] "v"(c.qf[2][3]), [s102] "v"(S[1][0][2]), [s103] "v"(S[1][0][3])
        : "memory", "m0", "scc", ACLOB);
    asm volatile("v_mfma_f32_16x16x32_bf16 %[S30], a[144:147], %[q30], %[negm3]\n\t"
        "v_exp_f32_e32 %[e14], %[s110]\n\t"
        "v_mfma_f32_16x16x32_bf16 %[S31], a[160:163], %[q30], %[negm3]\n\t"
        "v_exp_f32_e32 %[e15], %[s111]\n\t"
        "v_mfma_f32_16x16x32_bf16 %[S30], a[148:151], %[q31], %[S30]\n\t"
        "v_exp_f32_e32 %[e16], %[s112]\n\t"
        "v_mfma_f32_16x16x32_bf16 %[S31], a[164:167], %[q31], %[S31]\n\t"
        "v_exp_f32_e32 %[e17], %[s113]\n\t"
        "v_mfma_f32_16x16x32_bf16 %[S30], a[152:155], %[q32], %[S30]\n\t"
        "v_cvt_pk_bf16_f32 %[pw10], %[e10], %[e11]\n\t"
        "v_cvt_pk_bf16_f32 %[pw11], %[e12], %[e13]\n\t"
        "v_mfma_f32_16x16x32_bf16 %[S31], a[168:171], %[q32], %[S31]\n\t"
        "v_cvt_pk_bf16_f32 %[pw12], %[e14], %[e15]\n\t"
        "v_cvt_pk_bf16_f32 %[pw13], %[e16], %[e17]\n\t"
        "v_mfma_f32_16x16x32_bf16 %[S30], a[156:159], %[q33], %[S30]\n\t"
        "v_exp_f32_e32 %[e20], %[s200]\n\t"
        "v_mfma_f32_16x16x32_bf16 %[S31], a[172:175], %[q33], %[S31]\n\t"
        "v_exp_f32_e32 %[e21], %[s201]"
        : [S30] "=&v"(S[3][0]), [e14] "=&v"(E[1][4]), [S31] "=&v"(S[3][1]), [e15] "=&v"(E[1][5]), [e16] "=&v"(E[1][6]), [e17] "=&v"(E[1][7]), [pw10] "=&v"(PW[1][0]), [pw11] "=&v"(PW[1][1]), [pw12] "=&v"(PW[1][2]), [pw13] "=&v"(PW[1][3]), [e20] "=&v"(E[2][0]), [e21] "=&v"(E[2][1])
        : [q30] "v"(c.qf[3][0]), [negm3] "v"(c.negm[3]), [s110] "v"(S[1][1][0]), [s111] "v"(S[1][1][1]), [q31] "v"(c.qf[3][1]), [s112] "v"(S[1][1][2]), [s113] "v"(S[1][1][3]), [q32] "v"(c.qf[3][2]), [e10] "v"(E[1][0]), [e11] "v"(E[1][1]), [e12] "v"(E[1][2]), [e13] "v"(E[1][3]), [q33] "v"(c.qf[3][3]), [s200] "v"(S[2][0][0]), [s201] "v"(S[2][0][1])
        : "memory", "m0", "scc", ACLOB);
    asm volatile("s_waitcnt lgkmcnt(0)\n\t"
        "s_nop 1\n\t"
        "v_mfma_f32_16x16x32_bf16 a[0:3], a[176:179], %[p0], a[0:3]\n\t"
        "v_exp_f32_e32 %[e22], %[s202]\n\t"
        "v_mfma_f32_16x16x32_bf16 a[16:19], a[180:183], %[p0], a[16:19]\n\t"
        "v_exp_f32_e32 %[e23], %[s203]\n\t"
        "v_mfma_f32_16x16x32_bf16 a[32:35], a[184:187], %[p0], a[32:35]\n\t"
        "v_exp_f32_e32 %[e24], %[s210]\n\t"
        "v_mfma_f32_16x16x32_bf16 a[48:51], a[188:191], %[p0], a[48:51]\n\t"
        "v_exp_f32_e32 %[e25], %[s211]\n\t"
        "v_mfma_f32_16x16x32_bf16 a[64:67], a[192:195], %[p0], a[64:67]\n\t"
        "v_exp_f32_e32 %[e26], %[s212]\n\t"
        "v_mfma_f32_16x16x32_bf16 a[80:83], a[196:199], %[p0], a[80:83]\n\t"
        "v_exp_f32_e32 %[e27], %[s213]\n\t"
        "v_mfma_f32_16x16x32_bf16 a[96:99], a[200:203], %[p0], a[96:99]\n\t"
        "v_cvt_pk_bf16_f32 %[pw20], %[e20], %[e21]\n\t"
        "v_cvt_pk_bf16_f32 %[pw21], %[e22], %[e23]\n\t"
        "v_mfma_f32_16x16x32_bf16 a[112:115], a[204:207], %[p0], a[112:115]\n\t"
        "v_cvt_pk_bf16_f32 %[pw22], %[e24], %[e25]\n\t"
        "v_cvt_pk_bf16_f32 %[pw23], %[e26], %[e27]\n\t"
        "v_mfma_f32_16x16x32_bf16 a[128:131], a[208:211], %[p0], a[128:131]\n\t"
        "v_exp_f32_e32 %[e30], %[s300]"
        : [e22] "=&v"(E[2][2]), [e23] "=&v"(E[2][3]), [e24] "=&v"(E[2][4]), [e25] "=&v"(E[2][5]), [e26] "=&v"(E[2][6]), [e27] "=&v"(E[2][7]), [pw20] "=&v"(PW[2][0]), [pw21] "=&v"(PW[2][1]), [pw22] "=&v"(PW[2][2]), [pw23] "=&v"(PW[2][3]), [e30] "=&v"(E[3][0])
        : [p0] "v"(mk8(PW[0][0], PW[0][1], PW[0][2], PW[0][3])), [s202] "v"(S[2][0][2]), [s203] "v"(S[2][0][3]), [s210] "v"(S[2][1][0]), [s211] "v"(S[2][1][1]), [s212] "v"(S[2][1][2]), [s213] "v"(S[2][1][3]), [e20] "v"(E[2][0]), [e21] "v"(E[2][1]), [s300] "v"(S[3][0][0])
        : "memory", "m0", "scc", ACLOB);
    asm volatile("s_nop 1\n\t"
        "v_mfma_f32_16x16x32_bf16 a[4:7], a[176:179], %[p1], a[4:7]\n\t"
        "v_exp_f32_e32 %[e31], %[s301]\n\t"
        "v_mfma_f32_16x16x32_bf16 a[20:23], a[180:183], %[p1], a[20:23]\n\t"
        "v_exp_f32_e32 %[e32], %[s302]\n\t"
        "v_mfma_f32_16x16x32_bf16 a[36:39], a[184:187], %[p1], a[36:39]\n\t"
        "v_exp_f32_e32 %[e33], %[s303]\n\t"
        "v_mfma_f32_16x16x32_bf16 a[52:55], a[188:191], %[p1], a[52:55]\n\t"
        "v_exp_f32_e32 %[e34], %[s310]\n\t"
        "v_mfma_f32_16x16x32_bf16 a[68:71], a[192:195], %[p1], a[68:71]\n\t"
        "v_exp_f32_e32 %[e35], %[s311]\n\t"
        "v_mfma_f32_16x16x32_bf16 a[84:87], a[196:199], %[p1], a[84:87]\n\t"
        "v_exp_f32_e32 %[e36], %[s312]\n\t"
        "v_mfma_f32_16x16x32_bf16 a[100:103], a[200:203], %[p1], a[100:103]\n\t"
        "v_exp_f32_e32 %[e37], %[s313]\n\t"
        "v_mfma_f32_16x16x32_bf16 a[116:119], a[204:207], %[p1], a[116:119]\n\t"
        "v_cvt_pk_bf16_f32 %[pw30], %[e30], %[e31]\n\t"
        "v_cvt_pk_bf16_f32 %[pw31], %[e32], %[e33]\n\t"
        "v_mfma_f32_16x16x32_bf16 a[132:135], a[208:211], %[p1], a[132:135]\n\t"
        "v_cvt_pk_bf16_f32 %[pw32], %[e34], %[e35]\n\t"
        "v_cvt_pk_bf16_f32 %[pw33], %[e36], %[e37]"
        : [e31] "=&v"(E[3][1]), [e32] "=&v"(E[3][2]), [e33] "=&v"(E[3][3]), [e34] "=&v"(E[3][4]), [e35] "=&v"(E[3][5]), [e36] "=&v"(E[3][6]), [e37] "=&v"(E[3][7]), [pw30] "=&v"(PW[3][0]), [pw31] "=&v"(PW[3][1]), [pw32] "=&v"(PW[3][2]), [pw33] "=&v"(PW[3][3])
        : [p1] "v"(mk8(PW[1][0], PW[1][1], PW[1][2], PW[1][3])), [s301] "v"(S[3][0][1]), [s302] "v"(S[3][0][2]), [s303] "v"(S[3][0][3]), [s310] "v"(S[3][1][0]), [s311] "v"(S[3][1][1]), [s312] "v"(S[3][1][2]), [s313] "v"(S[3][1][3]), [e30] "v"(E[3][0])
        : "memory", "m0", "scc", ACLOB);
    asm volatile("s_nop 1\n\t"
        "v_mfma_f32_16x16x32_bf16 a[8:11], a[176:179], %[p2], a[8:11]\n\t"
        "ds_read_b128 a[144:147], %[kan] offset:0\n\t"
        "ds_read_b128 a[148:151], %[kan] offset:1024\n\t"
        "v_mfma_f32_16x16x32_bf16 a[24:27], a[180:183], %[p2], a[24:27]\n\t"
        "ds_read_b128 a[152:155], %[kan] offset:2048\n\t"
        "ds_read_b128 a[156:159], %[kan] offset:3072\n\t"
        "v_mfma_f32_16x16x32_bf16 a[40:43], a[184:187], %[p2], a[40:43]\n\t"
        "ds_read_b128 a[160:163], %[kan] offset:4096\n\t"
        "ds_read_b128 a[164:167], %[kan] offset:5120\n\t"
        "v_mfma_f32_16x16x32_bf16 a[56:59], a[188:191], %[p2], a[56:59]\n\t"
        "ds_read_b128 a[168:171], %[kan] offset:6144\n\t"
        "ds_read_b128 a[172:175], %[kan] offset:7168\n\t"
        "v_mfma_f32_16x16x32_bf16 a[72:75], a[192:195], %[p2], a[72:75]\n\t"
        "v_mfma_f32_16x16x32_bf16 a[88:91], a[196:199], %[p2], a[88:91]\n\t"
        "v_mfma_f32_16x16x32_bf16 a[104:107], a[200:203], %[p2], a[104:107]\n\t"
        "v_mfma_f32_16x16x32_bf16 a[120:123], a[204:207], %[p2], a[120:123]\n\t"
        "v_mfma_f32_16x16x32_bf16 a[136:139], a[208:211], %[p2], a[136:139]"
        :
        : [p2] "v"(mk8(PW[2][0], PW[2][1], PW[2][2], PW[2][3])), [kan] "v"(c.kan)
        : "memory", "m0", "scc", ACLOB);
    asm volatile("s_nop 1\n\t"
        "v_mfma_f32_16x16x32_bf16 a[12:15], a[176:179], %[p3], a[12:15]\n\t"
        "v_mfma_f32_16x16x32_bf16 a[28:31], a[180:183], %[p3], a[28:31]\n\t"
        "v_mfma_f32_16x16x32_bf16 a[44:47], a[184:187], %[p3], a[44:47]\n\t"
        "v_mfma_f32_16x16x32_bf16 a[60:63], a[188:191], %[p3], a[60:63]\n\t"
        "v_mfma_f32_16x16x32_bf16 a[76:79], a[192:195], %[p3], a[76:79]\n\t"
        "v_mfma_f32_16x16x32_bf16 a[92:95], a[196:199], %[p3], a[92:95]\n\t"
        "v_mfma_f32_16x16x32_bf16 a[108:111], a[200:203], %[p3], a[108:111]\n\t"
        "v_mfma_f32_16x16x32_bf16 a[124:127], a[204:207], %[p3], a[124:127]\n\t"
        "v_mfma_f32_16x16x32_bf16 a[140:143], a[208:211], %[p3], a[140:143]"
        :
        : [p3] "v"(mk8(PW[3][0], PW[3][1], PW[3][2], PW[3][3]))
        : "memory", "m0", "scc", ACLOB);
}
DI void att_half_1dx(AttC& c) {
    f32x4 S[4][2]; float E[4][8]; unsigned PW[4][4];
    asm volatile("s_waitcnt vmcnt(10) lgkmcnt(0)\n\t"
        "s_barrier\n\t"
        "s_nop 7\n\t"
        "ds_read_b64_tr_b16 a[176:177], %[vb0] offset:8192\n\t"
        "s_nop 0\n\t"
        "ds_read_b64_tr_b16 a[178:179], %[vb0] offset:12288\n\t"
        "s_nop 0\n\t"
        "ds_read_b64_tr_b16 a[180:181], %[vb1] offset:8192\n\t"
        "s_nop 0\n\t"
        "ds_read_b64_tr_b16 a[182:183], %[vb1] offset:12288\n\t"
        "s_nop 0\n\t"
        "ds_read_b64_tr_b16 a[184:185], %[vb0] offset:9216\n\t"
        "s_nop 0\n\t"
        "ds_read_b64_tr_b16 a[186:187], %[vb0] offset:13312\n\t"
        "s_nop 0\n\t"
        "ds_read_b64_tr_b16 a[188:189], %[vb1] offset:9216\n\t"
        "s_nop 0\n\t"
        "ds_read_b64_tr_b16 a[190:191], %[vb1] offset:13312\n\t"
        "s_nop 0"
        :
        : [vb0] "v"(c.vb[0]), [vb1] "v"(c.vb[1])
        : "memory", "m0", "scc", ACLOB);
    asm volatile("s_nop 7\n\t"
        "ds_read_b64_tr_b16 a[192:193], %[vb0] offset:10240\n\t"
        "s_nop 0\n\t"
        "ds_read_b64_tr_b16 a[194:195], %[vb0] offset:14336\n\t"
        "s_nop 0\n\t"
        "ds_read_b64_tr_b16 a[196:197], %[vb1] offset:10240\n\t"
        "s_nop 0\n\t"
        "ds_read_b64_tr_b16 a[198:199], %[vb1] offset:14336\n\t"
        "s_nop 0\n\t"
        "ds_read_b64_tr_b16 a[200:201], %[vb0] offset:11264\n\t"
        "s_nop 0\n\t"
        "ds_read_b64_tr_b16 a[202:203], %[vb0] offset:15360\n\t"
        "s_nop 0\n\t"
        "ds_read_b64_tr_b16 a[204:205], %[vb1] offset:11264\n\t"
        "s_nop 0\n\t"
        "ds_read_b64_tr_b16 a[206:207], %[vb1] offset:15360\n\t"
        "s_nop 0"
        :
        : [vb0] "v"(c.vb[0]), [vb1] "v"(c.vb[1])
        : "memory", "m0", "scc", ACLOB);
    asm volatile("v_mfma_f32_16x16x32_bf16 %[S20], a[144:147], %[q20], %[negm2]\n\t"
        "v_mfma_f32_16x16x32_bf16 %[S20], a[148:151], %[q21], %[S20]\n\t"
        "v_mfma_f32_16x16x32_bf16 %[S20], a[152:155], %[q22], %[S20]\n\t"
        "v_mfma_f32_16x16x32_bf16 %[S20], a[156:159], %[q23], %[S20]\n\t"
        "s_nop 7"
        : [S20] "=&v"(S[2][0])
        : [q20] "v"(c.qf[2][0]), [negm2] "v"(c.negm[2]), [q21] "v"(c.qf[2][1]), [q22] "v"(c.qf[2][2]), [q23] "v"(c.qf[2][3])
        : "memory", "m0", "scc", ACLOB);
    asm volatile("v_mfma_f32_16x16x32_bf16 %[S30], a[144:147], %[q30], %[negm3]\n\t"
        "v_mfma_f32_16x16x32_bf16 %[S31], a[160:163], %[q30], %[negm3]\n\t"
        "v_mfma_f32_16x16x32_bf16 %[S30], a[148:151], %[q31], %[S30]\n\t"
        "v_cndmask_b32_e64 %[e20], %[s200], %[ninf], %[mk0]\n\t"
        "v_exp_f32_e32 %[e20], %[e20]\n\t"
        "v_mfma_f32_16x16x32_bf16 %[S31], a[164:167], %[q31], %[S31]\n\t"
        "v_cndmask_b32_e64 %[e21], %[s201], %[ninf], %[mk1]\n\t"
        "v_exp_f32_e32 %[e21], %[e21]\n\t"
        "v_mfma_f32_16x16x32_bf16 %[S30], a[152:155], %[q32], %[S30]\n\t"
        "v_cndmask_b32_e64 %[e22], %[s202], %[ninf], %[mk2]\n\t"
        "v_exp_f32_e32 %[e22], %[e22]\n\t"
        "v_mfma_f32_16x16x32_bf16 %[S31], a[168:171], %[q32], %[S31]\n\t"
        "v_cndmask_b32_e64 %[e23], %[s203], %[ninf], %[mk3]\n\t"
        "v_exp_f32_e32 %[e23], %[e23]\n\t"
        "v_mfma_f32_16x16x32_bf16 %[S30], a[156:159], %[q33], %[S30]\n\t"
        "v_cvt_pk_bf16_f32 %[pw20], %[e20], %[e21]\n\t"
        "v_cvt_pk_bf16_f32 %[pw21], %[e22], %[e23]\n\t"
        "v_mfma_f32_16x16x32_bf16 %[S31], a[172:175], %[q33], %[S31]\n\t"
        "v_mov_b32_e32 %[pw22], 0\n\t"
        "v_mov_b32_e32 %[pw23], 0"
        : [S30] "=&v"(S[3][0]), [S31] "=&v"(S[3][1]), [e20] "=&v"(E[2][0]), [e21] "=&v"(E[2][1]), [e22] "=&v"(E[2][2]), [e23] "=&v"(E[2][3]), [pw20] "=&v"(PW[2][0]), [pw21] "=&v"(PW[2][1]), [pw22] "=&v"(PW[2][2]), [pw23] "=&v"(PW[2][3])
        : [q30] "v"(c.qf[3][0]), [negm3] "v"(c.negm[3]), [q31] "v"(c.qf[3][1]), [s200] "v"(S[2][0][0]), [ninf] "v"(c.ninf), [mk0] "s"(c.mask[0]), [s201] "v"(S[2][0][1]), [mk1] "s"(c.mask[1]), [q32] "v"(c.qf[3][2]), [s202] "v"(S[2][0][2]), [mk2] "s"(c.mask[2]), [s203] "v"(S[2][0][3]), [mk3] "s"(c.mask[3]), [q33] "v"(c.qf[3][3])
        : "memory", "m0", "scc", ACLOB);
    asm volatile("s_waitcnt lgkmcnt(0)\n\t"
        "s_nop 7\n\t"
        "v_exp_f32_e32 %[e30], %[s300]\n\t"
        "s_nop 0\n\t"
        "v_exp_f32_e32 %[e31], %[s301]\n\t"
        "s_nop 0"
        : [e30] "=&v"(E[3][0]), [e31] "=&v"(E[3][1])
        : [s300] "v"(S[3][0][0]), [s301] "v"(S[3][0][1])
        : "memory", "m0", "scc", ACLOB);
    asm volatile("s_nop 7\n\t"
        "v_exp_f32_e32 %[e32], %[s302]\n\t"
        "s_nop 0\n\t"
        "v_exp_f32_e32 %[e33], %[s303]\n\t"
        "s_nop 0\n\t"
        "v_cndmask_b32_e64 %[e34], %[s310], %[ninf], %[mk0]\n\t"
        "v_exp_f32_e32 %[e34], %[e34]\n\t"
        "s_nop 0\n\t"
        "v_cndmask_b32_e64 %[e35], %[s311], %[ninf], %[mk1]\n\t"
        "v_exp_f32_e32 %[e35], %[e35]\n\t"
        "s_nop 0"
        : [e32] "=&v"(E[3][2]), [e33] "=&v"(E[3][3]), [e34] "=&v"(E[3][4]), [e35] "=&v"(E[3][5])
        : [s302] "v"(S[3][0][2]), [s303] "v"(S[3][0][3]), [s310] "v"(S[3][1][0]), [ninf] "v"(c.ninf), [mk0] "s"(c.mask[0]), [s311] "v"(S[3][1][1]), [mk1] "s"(c.mask[1])
        : "memory", "m0", "scc", ACLOB);
    asm volatile("s_nop 1\n\t"
        "v_mfma_f32_16x16x32_bf16 a[8:11], a[176:179], %[p2], a[8:11]\n\t"
        "v_cndmask_b32_e64 %[e36], %[s312], %[ninf], %[mk2]\n\t"
        "v_exp_f32_e32 %[e36], %[e36]\n\t"
        "v_mfma_f32_16x16x32_bf16 a[24:27], a[180:183], %[p2], a[24:27]\n\t"
        "v_cndmask_b32_e64 %[e37], %[s313], %[ninf], %[mk3]\n\t"
        "v_exp_f32_e32 %[e37], %[e37]\n\t"
        "v_mfma_f32_16x16x32_bf16 a[40:43], a[184:187], %[p2], a[40:43]\n\t"
        "v_cvt_pk_bf16_f32 %[pw30], %[e30], %[e31]\n\t"
        "v_cvt_pk_bf16_f32 %[pw31], %[e32], %[e33]\n\t"
        "v_mfma_f32_16x16x32_bf16 a[56:59], a[188:191], %[p2], a[56:59]\n\t"
        "v_cvt_pk_bf16_f32 %[pw32], %[e34], %[e35]\n\t"
        "v_cvt_pk_bf16_f32 %[pw33], %[e36], %[e37]\n\t"
        "v_mfma_f32_16x16x32_bf16 a[72:75], a[192:195], %[p2], a[72:75]\n\t"
        "ds_read_b128 a[144:147], %[kan] offset:0\n\t"
        "ds_read_b128 a[148:151], %[kan] offset:1024\n\t"
        "v_mfma_f32_16x16x32_bf16 a[88:91], a[196:199], %[p2], a[88:91]\n\t"
        "ds_read_b128 a[152:155], %[kan] offset:2048\n\t"
        "ds_read_b128 a[156:159], %[kan] offset:3072\n\t"
        "v_mfma_f32_16x16x32_bf16 a[104:107], a[200:203], %[p2], a[104:107]\n\t"
        "ds_read_b128 a[160:163], %[kan] offset:4096\n\t"
        "ds_read_b128 a[164:167], %[kan] offset:5120\n\t"
        "v_mfma_f32_16x16x32_bf16 a[120:123], a[204:207], %[p2], a[120:123]\n\t"
        "ds_read_b128 a[168:171], %[kan] offset:6144\n\t"
        "ds_read_b128 a[172:175], %[kan] offset:7168\n\t"
        "v_mfma_f32_16x16x32_bf16 a[136:139], a[208:211], %[p2], a[136:139]"
        : [e36] "=&v"(E[3][6]), [e37] "=&v"(E[3][7]), [pw30] "=&v"(PW[3][0]), [pw31] "=&v"(PW[3][1]), [pw32] "=&v"(PW[3][2]), [pw33] "=&v"(PW[3][3])
        : [p2] "v"(mk8(PW[2][0], PW[2][1], PW[2][2], PW[2][3])), [s312] "v"(S[3][1][2]), [ninf] "v"(c.ninf), [mk2] "s"(c.mask[2]), [s313] "v"(S[3][1][3]), [mk3] "s"(c.mask[3]), [e30] "v"(E[3][0]), [e31] "v"(E[3][1]), [e32] "v"(E[3][2]), [e33] "v"(E[3][3]), [e34] "v"(E[3][4]), [e35] "v"(E[3][5]), [kan] "v"(c.kan)
        : "memory", "m0", "scc", ACLOB);
    asm volatile("s_nop 1\n\t"
        "v_mfma_f32_16x16x32_bf16 a[12:15], a[176:179], %[p3], a[12:15]\n\t"
        "v_mfma_f32_16x16x32_bf16 a[28:31], a[180:183], %[p3], a[28:31]\n\t"
        "v_mfma_f32_16x16x32_bf16 a[44:47], a[184:187], %[p3], a[44:47]\n\t"
        "v_mfma_f32_16x16x32_bf16 a[60:63], a[188:191], %[p3], a[60:63]\n\t"
        "v_mfma_f32_16x16x32_bf16 a[76:79], a[192:195], %[p3], a[76:79]\n\t"
        "v_mfma_f32_16x16x32_bf16 a[92:95], a[196:199], %[p3], a[92:95]\n\t"
        "v_mfma_f32_16x16x32_bf16 a[108:111], a[200:203], %[p3], a[108:111]\n\t"
        "v_mfma_f32_16x16x32_bf16 a[124:127], a[204:207], %[p3], a[124:127]\n\t"
        "v_mfma_f32_16x16x32_bf16 a[140:143], a[208:211], %[p3], a[140:143]"
        :
        : [p3] "v"(mk8(PW[3][0], PW[3][1], PW[3][2], PW[3][3]))
        : "memory", "m0", "scc", ACLOB);
}
DI void att_half_1sx(AttC& c) {
    f32x4 S[4][2]; float E[4][8]; unsigned PW[4][4];
    asm volatile("s_waitcnt vmcnt(10) lgkmcnt(0)\n\t"
        "s_barrier"
        :
        :
        : "memory", "m0", "scc", ACLOB);
    asm volatile("s_waitcnt lgkmcnt(0)"
        :
        :
        : "memory", "m0", "scc", ACLOB);
    asm volatile("ds_read_b128 a[144:147], %[kan] offset:0\n\t"
        "s_nop 0\n\t"
        "ds_read_b128 a[148:151], %[kan] offset:1024\n\t"
        "s_nop 0\n\t"
        "ds_read_b128 a[152:155], %[kan] offset:2048\n\t"
        "s_nop 0\n\t"
        "ds_read_b128 a[156:159], %[kan] offset:3072\n\t"
        "s_nop 0\n\t"
        "ds_read_b128 a[160:163], %[kan] offset:4096\n\t"
        "s_nop 0\n\t"
        "ds_read_b128 a[164:167], %[kan] offset:5120\n\t"
        "s_nop 0\n\t"
        "ds_read_b128 a[168:171], %[kan] offset:6144\n\t"
        "s_nop 0\n\t"
        "ds_read_b128 a[172:175], %[kan] offset:7168\n\t"
        "s_nop 0"
        :
        : [kan] "v"(c.kan)
        : "memory", "m0", "scc", ACLOB);
}

DI void dma4(unsigned m0v, unsigned voff, const char* sb) {
    asm volatile("s_mov_b32 m0, %0\n\ts_nop 4\n\tglobal_load_lds_dwordx4 %1, %2 offset:0\n\tglobal_load_lds_dwordx4 %1, %2 offset:1024\n\tglobal_load_lds_dwordx4 %1, %2 offset:2048\n\tglobal_load_lds_dwordx4 %1, %2 offset:3072"
                 :: "s"(m0v), "v"(voff), "s"(sb) : "memory", "m0");
}
template <int R> DI void acc_zero4() { asm volatile("v_accvgpr_write_b32 a%c0, 0\n\tv_accvgpr_write_b32 a%c1, 0\n\tv_accvgpr_write_b32 a%c2, 0\n\tv_accvgpr_write_b32 a%c3, 0" :: "i"(R), "i"(R + 1), "i"(R + 2), "i"(R + 3) : ACLOB); }
template <int... Is> DI void acc_zero_all(std::integer_sequence<int, Is...>) { (acc_zero4<4 * Is>(), ...); }
template <int R> DI f32x4 acc_rd4() { f32x4 v;
    asm volatile("v_accvgpr_read_b32 %0, a%c4\n\tv_accvgpr_read_b32 %1, a%c5\n\tv_accvgpr_read_b32 %2, a%c6\n\tv_accvgpr_read_b32 %3, a%c7"
                 : "=v"(v[0]), "=v"(v[1]), "=v"(v[2]), "=v"(v[3]) : "i"(R), "i"(R + 1), "i"(R + 2), "i"(R + 3) : ACLOB);
    return v; }
template <int HF> DI void kfrag_load(unsigned ka) {
    asm volatile("ds_read_b128 a[144:147], %0 offset:%c1\n\tds_read_b128 a[148:151], %0 offset:%c1+1024\n\tds_read_b128 a[152:155], %0 offset:%c1+2048\n\tds_read_b128 a[156:159], %0 offset:%c1+3072\n\t"
                 "ds_read_b128 a[160:163], %0 offset:%c1+4096\n\tds_read_b128 a[164:167], %0 offset:%c1+5120\n\tds_read_b128 a[168:171], %0 offset:%c1+6144\n\tds_read_b128 a[172:175], %0 offset:%c1+7168"
                 :: "v"(ka), "i"(HF * 8192) : "memory", ACLOB);
}
DI void qk_raw(f32x4& s0, f32x4& s1, const bf16x8 (&q)[4]) {
    asm volatile("s_waitcnt lgkmcnt(0)\n\t"
        "v_mfma_f32_16x16x32_bf16 %0, a[144:147], %2, 0\n\tv_mfma_f32_16x16x32_bf16 %1, a[160:163], %2, 0\n\t"
        "v_mfma_f32_16x16x32_bf16 %0, a[148:151], %3, %0\n\tv_mfma_f32_16x16x32_bf16 %1, a[164:167], %3, %1\n\t"
        "v_mfma_f32_16x16x32_bf16 %0, a[152:155], %4, %0\n\tv_mfma_f32_16x16x32_bf16 %1, a[168:171], %4, %1\n\t"
        "v_mfma_f32_16x16x32_bf16 %0, a[156:159], %5, %0\n\tv_mfma_f32_16x16x32_bf16 %1, a[172:175], %5, %1"
        : "=&v"(s0), "=&v"(s1) : "v"(q[0]), "v"(q[1]), "v"(q[2]), "v"(q[3]) : "memory", ACLOB);
}

template <int CC, int I> DI void att_out1(LAS unsigned char* xo, int lane) {
    constexpr int db = CC ? (I >> 2) : 4 + (I >> 2), qi = I & 3;
    *(LAS f32x4*)(xo + I * 1024 + lane * 16) = acc_rd4<4 * (db * 4 + qi)>();
}
template <int CC, int I> DI void att_fin1(const f32x4 (&xr)[16], rsrc_t rs, unsigned dst, const float (&a1)[4], const float (&a2)[4], bool odd) {
    constexpr int pr = I >> 2, qi = I & 3, db = 4 * CC + 2 * pr;
    const f32x4 y0 = acc_rd4<4 * (db * 4 + qi)>() * a1[qi] + xr[(2 * pr) * 4 + qi] * a2[qi], y1 = acc_rd4<4 * ((db + 1) * 4 + qi)>() * a1[qi] + xr[(2 * pr + 1) * 4 + qi] * a2[qi];
    u32x4 w; const unsigned p0 = cvtpk(y0[0], y0[1]), p1 = cvtpk(y0[2], y0[3]), p2 = cvtpk(y1[0], y1[1]), p3 = cvtpk(y1[2], y1[3]);
    w[0] = odd ? p2 : p0; w[1] = odd ? p3 : p1; w[2] = odd ? p0 : p2; w[3] = odd ? p1 : p3;
    st16(rs, dst + (unsigned)((qi * 240 + pr) * 1024), w);
}
template <int CC, int... Is, int... Js> DI void att_merge(LAS unsigned char* lds, rsrc_t rs, unsigned dst, const float m0, const float m1, const float m2, const float m3, int sl, int wid, int lane, std::integer_sequence<int, Is...>, std::integer_sequence<int, Js...>) {
    const int pw = wid ^ 1;
    LAS unsigned char* xo = lds + (wid < 2 ? 65536 + sl * 32768 + wid * 16384 : 131072 + (wid - 2) * 16384);
    const LAS unsigned char* xi = lds + (pw < 2 ? 65536 + sl * 32768 + pw * 16384 : 131072 + (pw - 2) * 16384);
    LAS f32x2* st = (LAS f32x2*)(lds + sl * 32768) + wid * 64;
    const LAS f32x2* sp = (const LAS f32x2*)(lds + sl * 32768) + pw * 64;
    const float mrow[4] = {m0, m1, m2, m3};
    float lr[4]; lr[0] = acc_rd4<128>()[0]; lr[1] = acc_rd4<132>()[0]; lr[2] = acc_rd4<136>()[0]; lr[3] = acc_rd4<140>()[0];
    if (lane < 16) { st[lane] = (f32x2){m0, lr[0]}; st[16 + lane] = (f32x2){m1, lr[1]}; st[32 + lane] = (f32x2){m2, lr[2]}; st[48 + lane] = (f32x2){m3, lr[3]}; }
    (att_out1<CC, Is>(xo, lane), ...);
    asm volatile("s_waitcnt lgkmcnt(0)" ::: "memory"); BAR; asm volatile("" ::: "memory");
    f32x2 o2[4]; f32x4 xr[16];
#pragma unroll
    for (int qi = 0; qi < 4; ++qi) o2[qi] = sp[qi * 16 + (lane & 15)];
#pragma unroll
    for (int i = 0; i < 16; ++i) xr[i] = *(const LAS f32x4*)(xi + i * 1024 + lane * 16);
    float a1[4], a2[4];
#pragma unroll
    for (int qi = 0; qi < 4; ++qi) {
        const float M = fmaxf(mrow[qi], o2[qi].x), f1 = __builtin_amdgcn_exp2f(mrow[qi] - M), f2 = __builtin_amdgcn_exp2f(o2[qi].x - M);
        const float inv = __builtin_amdgcn_rcpf(lr[qi] * f1 + o2[qi].y * f2); a1[qi] = f1 * inv; a2[qi] = f2 * inv;
    }
    (att_fin1<CC, Js>(xr, rs, dst, a1, a2, (lane >> 4) & 1), ...);
}
DI float xmax16_32(float v) {
    auto r = __builtin_amdgcn_permlane32_swap(__float_as_uint(v), __float_as_uint(v), false, false);
    v = fmaxf(__uint_as_float(r[0]), __uint_as_float(r[1]));
    auto s = __builtin_amdgcn_permlane16_swap(__float_as_uint(v), __float_as_uint(v), false, false);
    return fmaxf(__uint_as_float(s[0]), __uint_as_float(s[1]));
}
DI void att_load_q(AttC& ac, const unsigned char* ws, int h, int qb, int g) {
    const char* Qg = (const char*)ws + WS_QF + ((size_t)h * 128 + qb * 8 + g * 4) * 4096;
#pragma unroll
    for (int qi = 0; qi < 4; ++qi)
        asm volatile("s_nop 4\n\tglobal_load_dwordx4 %0, %4, %5 offset:0 sc1\n\tglobal_load_dwordx4 %1, %4, %5 offset:1024 sc1\n\tglobal_load_dwordx4 %2, %4, %5 offset:2048 sc1\n\tglobal_load_dwordx4 %3, %4, %5 offset:3072 sc1"
                     : "=&v"(ac.qf[qi][0]), "=&v"(ac.qf[qi][1]), "=&v"(ac.qf[qi][2]), "=&v"(ac.qf[qi][3]) : "v"(ac.dvo[0]), "s"(Qg + qi * 4096) : "memory");
}
#define ATT_WAIT_Q(N) asm volatile("s_waitcnt vmcnt(" #N ")" : "+v"(ac.qf[0][0]), "+v"(ac.qf[0][1]), "+v"(ac.qf[0][2]), "+v"(ac.qf[0][3]), "+v"(ac.qf[1][0]), "+v"(ac.qf[1][1]), "+v"(ac.qf[1][2]), "+v"(ac.qf[1][3]), \
                 "+v"(ac.qf[2][0]), "+v"(ac.qf[2][1]), "+v"(ac.qf[2][2]), "+v"(ac.qf[2][3]), "+v"(ac.qf[3][0]), "+v"(ac.qf[3][1]), "+v"(ac.qf[3][2]), "+v"(ac.qf[3][3]) :: "memory")

#define STAMP2(i) do {} while (0)
DI void attn_pair(LAS unsigned char* lds, unsigned char* ws, const int h, const int qbA, const int qbB, const int wid, int lane, const unsigned tag, const char* pf0, const char* pf1, const int hidx, const bool primed, const int qbidx, const char* codep) {
    const int g = wid >> 1, c = wid & 1, fr = lane & 15, fq = lane >> 4;
    const unsigned l0 = (unsigned)(size_t)lds;
    const char* Kg = (const char*)ws + WS_KF + (size_t)h * 128 * 4096 + wid * 8192;
    const char* Vg = (const char*)ws + WS_VF + (size_t)h * 32 * 16384 + wid * 8192;
    const unsigned mK = l0 + wid * 8192, mV = l0 + 65536 + wid * 8192;
    const int nA = qbA + 1;
    AttC ac; ac.dvo[0] = lane * 16; ac.dvo[1] = lane * 16 + 4096;
#define DMA8(m0v, src) do { dma4((m0v), ac.dvo[0], (src)); dma4((m0v) + 4096, ac.dvo[0], (src) + 4096); } while (0)
#define PFX() asm volatile("s_nop 4\n\tglobal_load_dwordx4 a[252:255], %0, %1" :: "v"(ac.dvo[0]), "s"(pf0) : "memory", ACLOB)
#define TILE(s) ((s) < nA ? (s) : ((s) - nA <= qbB ? (s) - nA : 0))
#define PRIME_DMA(s0) do { const unsigned po = (unsigned)((s0) & 1) * 32768u; \
        DMA8(mK + po, Kg + (size_t)TILE(s0) * 32768); DMA8(mV + po, Vg + (size_t)TILE(s0) * 32768); PFX(); DMA8(mK + (po ^ 32768u), Kg + (size_t)TILE((s0) + 1) * 32768); PFX(); } while (0)
#define PRIME_ADDR(s0) do { const unsigned po = (unsigned)((s0) & 1) * 32768u; \
        const int l2 = opaque_lane(), q = (l2 >> 2) & 3, pp = l2 & 3, f2 = l2 >> 4; \
        const unsigned b0 = l0 + 65536u + po + (unsigned)c * 16384u + (unsigned)((4 * f2 + q) * 64 + ((pp ^ (f2 & 1)) * 16) + (pp & 1) * 8); \
        ac.vb[0] = b0; ac.vb[1] = b0 ^ 8u; \
        ac.ka = l0 + po + (unsigned)c * 16384u + (unsigned)l2 * 16u; ac.kan = ac.ka ^ 32768u; } while (0)
    if (!primed) PRIME_DMA(0);
    att_load_q(ac, ws, h, qbA, g);
    PRIME_ADDR(0);
    if (wid == 0) poll_issue(lds + 163584, ws, qbidx);
    asm volatile("v_accvgpr_write_b32 a208, %0\n\tv_accvgpr_write_b32 a209, %0\n\tv_accvgpr_write_b32 a210, %0\n\tv_accvgpr_write_b32 a211, %0" :: "v"(0x3f803f80u) : ACLOB);
    ac.ninf = -__builtin_inff();
#pragma unroll
    for (int j = 0; j < 4; ++j) ac.mask[j] = __builtin_amdgcn_ballot_w64(4 * fq + j > fr);
    acc_zero_all(std::make_integer_sequence<int, 36>{});
    ATT_WAIT_Q(0);
    BAR;
    kfrag_load<0>(ac.ka);
    int s = 0;
    for (int b = 0; b < 2; ++b) {
        const int qb = b ? qbB : qbA, s0 = s;
        const bool act = !(qb == 0 && c > g);
#pragma unroll
        for (int qi = 0; qi < 4; ++qi) ac.negm[qi] = (f32x4){1e30f, 1e30f, 1e30f, 1e30f};
        if (act) {
            const bool dg = (qb == 0 && c == g);
            f32x4 s0_[4], s1_[4];
#pragma unroll
            for (int qi = 0; qi < 4; ++qi) qk_raw(s0_[qi], s1_[qi], ac.qf[qi]);
            asm volatile("s_nop 7\n\ts_nop 3" : "+v"(s0_[0]), "+v"(s1_[0]), "+v"(s0_[1]), "+v"(s1_[1]), "+v"(s0_[2]), "+v"(s1_[2]), "+v"(s0_[3]), "+v"(s1_[3]));
#pragma unroll
            for (int qi = 0; qi < 4; ++qi) {
                if (dg) { const int l2 = opaque_lane(), d0 = 4 * (l2 >> 4) - (l2 & 15) - qi * 16;
#pragma unroll
                    for (int j = 0; j < 4; ++j) { if (d0 + j > 0) s0_[qi][j] = ac.ninf; if (d0 + 16 + j > 0) s1_[qi][j] = ac.ninf; } }
                float mx = fmaxf(fmaxf(fmaxf(s0_[qi][0], s0_[qi][1]), fmaxf(s0_[qi][2], s0_[qi][3])), fmaxf(fmaxf(s1_[qi][0], s1_[qi][1]), fmaxf(s1_[qi][2], s1_[qi][3])));
                mx = xmax16_32(mx);
                ac.negm[qi] = (f32x4){-mx, -mx, -mx, -mx};
            }
        }
        STAMP2(1 + 3 * b);
        if (CODEPF2 && b == 1) asm volatile("s_nop 4\n\tglobal_load_dwordx4 a[252:255], %0, %1" :: "v"(ac.dvo[0]), "s"(codep) : "memory", ACLOB);
        if (b == 1 && wid == 0) poll_issue(lds + 163584, ws, hidx);
        for (int exact = 0;; ++exact) {
            for (int t = 0; t <= qb; ++t, ++s) {
                const unsigned so = (unsigned)(s & 1) * 32768u;
                ac.dm0[0] = mV + (so ^ 32768u); ac.dsb[0] = Vg + (size_t)TILE(s + 1) * 32768;
                ac.dm0[1] = mK + so; ac.dsb[1] = Kg + (size_t)TILE(s + 2) * 32768;
                { const int pi_ = s < 8 ? s : 7; ac.pfs[0] = pf0 + pi_ * 4096; ac.pfs[1] = pf1 + (pi_ < 7 ? pi_ : 6) * 4096; }
                if (t < qb) { att_half_0n(ac); att_half_1n(ac); }
                else if (c < g) { att_half_0n(ac); att_half_1nx(ac); }
                else if (c == g) { att_half_0d(ac); att_half_1dx(ac); }
                else { att_half_0s(ac); att_half_1sx(ac); }
                ac.ka ^= 32768u; ac.kan ^= 32768u;
#pragma unroll
                for (int i = 0; i < 2; ++i) ac.vb[i] ^= 32768u;
                if (b == 1 && t == 0 && wid == 0 && lane == 0) flag_set(ws, W_C + ((qbA >> 1) * 4 + (h >> 3)) * 64 + (qbA & 1) * 8 + (h & 7), tag);
            }
            asm volatile("s_nop 7\n\ts_nop 7" ::: "memory");
            const float lmax = fmaxf(fmaxf(acc_rd4<128>()[0], acc_rd4<132>()[0]), fmaxf(acc_rd4<136>()[0], acc_rd4<140>()[0]));
            const float lsum = (acc_rd4<128>()[0] + acc_rd4<132>()[0]) + (acc_rd4<136>()[0] + acc_rd4<140>()[0]);
            const bool badw = __any(!(lmax < 1.1529215e18f) || lsum != lsum);
            LAS unsigned* bw = (LAS unsigned*)(lds + ((s - 1) & 1) * 32768 + 4096);
            if (lane == 0) bw[wid] = badw ? 1u : 0u;
            asm volatile("s_waitcnt lgkmcnt(0)" ::: "memory"); BAR; asm volatile("" ::: "memory");
            const unsigned redo = (unsigned)__builtin_amdgcn_readfirstlane(bw[0] | bw[1] | bw[2] | bw[3]);
            if (__builtin_expect(redo == 0u || exact, 1)) break;
            DRAIN_BAR();
            float mx[4] = {ac.ninf, ac.ninf, ac.ninf, ac.ninf};
            for (int t = 0; t <= qb; ++t) {
                DMA8(mK, Kg + (size_t)t * 32768);
                DRAIN_BAR();
                if (t < qb || c <= g) {
                    const bool dg = (t == qb && c == g); const int l2 = opaque_lane(); const unsigned kaf = l0 + (unsigned)c * 16384u + (unsigned)l2 * 16u;
#pragma unroll
                    for (int hf = 0; hf < 2; ++hf) {
                        if (hf) kfrag_load<1>(kaf); else kfrag_load<0>(kaf);
                        f32x4 s0_[4], s1_[4];
#pragma unroll
                        for (int qi = 0; qi < 4; ++qi) qk_raw(s0_[qi], s1_[qi], ac.qf[qi]);
                        asm volatile("s_nop 7\n\ts_nop 3" : "+v"(s0_[0]), "+v"(s1_[0]), "+v"(s0_[1]), "+v"(s1_[1]), "+v"(s0_[2]), "+v"(s1_[2]), "+v"(s0_[3]), "+v"(s1_[3]));
#pragma unroll
                        for (int qi = 0; qi < 4; ++qi) { const int d0 = 32 * hf + 4 * (l2 >> 4) - (l2 & 15) - qi * 16;
#pragma unroll
                            for (int j = 0; j < 4; ++j) { if (dg && d0 + j > 0) s0_[qi][j] = ac.ninf; if (dg && d0 + 16 + j > 0) s1_[qi][j] = ac.ninf;
                                mx[qi] = fmaxf(mx[qi], fmaxf(s0_[qi][j], s1_[qi][j])); } }
                    }
                }
                DRAIN_BAR();
            }
            if (act) {
#pragma unroll
                for (int qi = 0; qi < 4; ++qi) { const float m = xmax16_32(mx[qi]); ac.negm[qi] = (f32x4){-m, -m, -m, -m}; } }
            s = s0;
            PRIME_DMA(s0); PRIME_ADDR(s0);
            acc_zero_all(std::make_integer_sequence<int, 36>{});
            WAIT_V(18); BAR;
            kfrag_load<0>(ac.ka);
        }
        STAMP2(2 + 3 * b);
        if (b == 1 && wid == 0) { asm volatile("s_waitcnt vmcnt(0)" ::: "memory"); poll_finish_inv(lds + 163584, ws, hidx, tag); }
        if (b == 0) {
            if (wid == 0) { asm volatile("s_waitcnt vmcnt(8)" ::: "memory"); poll_finish(lds + 163584, ws, qbidx, tag); }
            BAR;
            att_load_q(ac, ws, h, qbB, g); }
        const int sl = (s - 1) & 1;
        const unsigned dst = (unsigned)(((qb * 8 + g * 4) * 240 + 4 * h + 2 * c) * 1024 + lane * 16); const rsrc_t rsA = mkrs(ws + WS_AF, 30u << 20);
        if (c) att_merge<1>(lds, rsA, dst, -ac.negm[0][0], -ac.negm[1][0], -ac.negm[2][0], -ac.negm[3][0], sl, wid, lane, std::make_integer_sequence<int, 16>{}, std::make_integer_sequence<int, 8>{});
        else att_merge<0>(lds, rsA, dst, -ac.negm[0][0], -ac.negm[1][0], -ac.negm[2][0], -ac.negm[3][0], sl, wid, lane, std::make_integer_sequence<int, 16>{}, std::make_integer_sequence<int, 8>{});
        if (b == 0) {
            acc_zero_all(std::make_integer_sequence<int, 36>{});
            asm volatile("s_waitcnt lgkmcnt(0)" ::: "memory"); BAR;
            DMA8(mK + sl * 32768u, Kg + (size_t)TILE(s + 1) * 32768); PFX();
            ATT_WAIT_Q(17);
        }
        STAMP2(3 + 3 * b);
    }
    DRAIN_BAR();
    if (wid == 0 && lane == 0) flag_set(ws, W_C + ((qbB >> 1) * 4 + (h >> 3)) * 64 + (qbB & 1) * 8 + (h & 7), tag);
#undef DMA8
#undef PFX
#undef PRIME_DMA
#undef PRIME_ADDR
#undef TILE
}

constexpr int S3 = 49152, K3_LDS = 3 * S3;
struct FragsB { s16x4 bl[4][2], bh[4][2]; };
#define BF3(n, k) __builtin_shufflevector(f.bl[n][k], f.bh[n][k], 0, 1, 2, 3, 4, 5, 6, 7)
#define M3(i, bop, aop) "v_mfma_f32_16x16x32_bf16 a[%c[r]+" #i "*4:%c[r]+" #i "*4+3], %[" bop "], %[" aop "], " CC(i) "\n\t"
#define ROW3_ASM(RA0, RA1, DMA0, DMA1, TAIL) \
        M3(0, "b00", "a0") RA0 \
        M3(1, "b10", "a0") "ds_read_b64_tr_b16 %[nl], %[bad] offset:%c[bo]\n\t" \
        M3(2, "b20", "a0") "ds_read_b64_tr_b16 %[nh], %[bad] offset:%c[bo]+512\n\t" \
        M3(3, "b30", "a0") DMA0 \
        M3(0, "b01", "a1") \
        M3(1, "b11", "a1") RA1 \
        M3(2, "b21", "a1") DMA1 \
        M3(3, "b31", "a1") TAIL
#define RA3_0 "ds_read_b128 %[pa0], %[aad] offset:%c[ao]-2048\n\t"
#define RA3_1 "ds_read_b128 %[pa1], %[aad] offset:%c[ao]-1024\n\t"
#define MID3 "ds_read_b128 %[a0], %[aad] offset:%c[ao]\n\t"
#define TAIL3 "ds_read_b128 %[a1], %[aad] offset:%c[ao]+1024\n\t"
#define DMA3(k) "s_add_u32 m0, %[ldsb], %c[mo" #k "]\n\ts_nop 0\n\tglobal_load_lds_dwordx4 %[vo" #k "], %[sb" #k "] offset:%c[go" #k "]\n\t"
template <int P> struct P3 { static constexpr bool isA = P < 8;
    static constexpr int go = isA ? (P & 1) * 1024 : ((P - 8) & 1) * 128;
    static constexpr int lds = isA ? (P >> 1) * 2048 + (P & 1) * 1024 : 0;
    static constexpr int ldsB = isA ? 0 : ((P - 8) >> 1) * 2048 + ((P - 8) & 1) * 1024;
};
template <int AO, int BO, int I> DI void g3_row(bf16x8 (&A)[8][2], const FragsB& f, FragsB& nx, unsigned aad, const unsigned (&bad)[4], unsigned ldsA, unsigned ldsB, const unsigned (&voffA)[4], const unsigned (&voffB)[2], const char* sa, const char* sb) {
    constexpr int P0 = I < 6 ? 2 * I : 0, P1 = I < 6 ? 2 * I + 1 : 0;
    constexpr int n = I >> 1, ks = I & 1, IP = I > 0 ? I - 1 : 0;
    using A0 = P3<P0>; using A1 = P3<P1>;
#define R3_B [b00] "v"(BF3(0, 0)), [b10] "v"(BF3(1, 0)), [b20] "v"(BF3(2, 0)), [b30] "v"(BF3(3, 0)), [b01] "v"(BF3(0, 1)), [b11] "v"(BF3(1, 1)), [b21] "v"(BF3(2, 1)), [b31] "v"(BF3(3, 1)), \
          [r] "i"(16 * I), [aad] "v"(aad), [ao] "i"(AO + I * 2048), [bad] "v"(bad[((n & 1) ? 1 : 0) + ((n >> 1) ? 2 : 0)]), [bo] "i"(BO + ks * 8192)
#define R3_D , [ldsb] "s"(A0::isA ? ldsA : ldsB), [mo0] "i"((A0::isA ? A0::lds : A0::ldsB) - A0::go), [vo0] "v"(A0::isA ? voffA[(P0 >> 1) & 3] : voffB[((P0 - 8) >> 1) & 1]), [sb0] "s"(A0::isA ? sa : sb), [go0] "i"(A0::go), \
          [mo1] "i"((A1::isA ? A1::lds : A1::ldsB) - A1::go), [vo1] "v"(A1::isA ? voffA[(P1 >> 1) & 3] : voffB[((P1 - 8) >> 1) & 1]), [sb1] "s"(A1::isA ? sa : sb), [go1] "i"(A1::go)
    if constexpr (I == 0)
        asm volatile(ROW3_ASM("", "", DMA3(0), DMA3(1), "") : [nl] "=&v"(nx.bl[n][ks]), [nh] "=&v"(nx.bh[n][ks]) : [a0] "v"(A[I][0]), [a1] "v"(A[I][1]), R3_B R3_D : "memory", "scc", "m0", ACLOB);
    else if constexpr (I < 6)
        asm volatile(ROW3_ASM(RA3_0, RA3_1, DMA3(0), DMA3(1), "") : [pa0] "+v"(A[IP][0]), [pa1] "+v"(A[IP][1]), [nl] "=&v"(nx.bl[n][ks]), [nh] "=&v"(nx.bh[n][ks]) : [a0] "v"(A[I][0]), [a1] "v"(A[I][1]), R3_B R3_D : "memory", "scc", "m0", ACLOB);
    else if constexpr (I == 6)
        asm volatile(ROW3_ASM(RA3_0, RA3_1, "", "", "") : [pa0] "+v"(A[IP][0]), [pa1] "+v"(A[IP][1]), [nl] "=&v"(nx.bl[n][ks]), [nh] "=&v"(nx.bh[n][ks]) : [a0] "v"(A[I][0]), [a1] "v"(A[I][1]), R3_B : "memory", ACLOB);
    else
        asm volatile(ROW3_ASM(RA3_0, RA3_1, "", MID3, TAIL3) : [pa0] "+v"(A[IP][0]), [pa1] "+v"(A[IP][1]), [nl] "=&v"(nx.bl[n][ks]), [nh] "=&v"(nx.bh[n][ks]), [a0] "+v"(A[I][0]), [a1] "+v"(A[I][1]) : R3_B : "memory", ACLOB);
}
template <int P> DI void g3_stage1(unsigned ldsA, unsigned ldsB, const unsigned (&voffA)[4], const unsigned (&voffB)[2], const char* sa, const char* sb) {
    using A0 = P3<P>;
    asm volatile("s_add_u32 m0, %[ldsb], %c[mo]\n\ts_nop 4\n\tglobal_load_lds_dwordx4 %[vo], %[sb] offset:%c[go]"
        :: [ldsb] "s"(A0::isA ? ldsA : ldsB), [mo] "i"((A0::isA ? A0::lds : A0::ldsB) - A0::go), [vo] "v"(A0::isA ? voffA[(P >> 1) & 3] : voffB[((P - 8) >> 1) & 1]), [sb] "s"(A0::isA ? sa : sb), [go] "i"(A0::go) : "memory", "scc", "m0");
}
template <int... Ps> DI void g3_stage(unsigned ldsA, unsigned ldsB, const unsigned (&voffA)[4], const unsigned (&voffB)[2], const char* sa, const char* sb, std::integer_sequence<int, Ps...>) { (g3_stage1<Ps>(ldsA, ldsB, voffA, voffB, sa, sb), ...); }
template <int I> DI void g3_read1(bf16x8 (&A)[8][2], FragsB& f, unsigned aad, const unsigned (&bad)[4]) {
    constexpr int n = I >> 1, ks = I & 1;
    RD128(A[I][0], aad, I * 2048); RD128(A[I][1], aad, I * 2048 + 1024);
    TRD(f.bl[n][ks], bad[((n & 1) ? 1 : 0) + ((n >> 1) ? 2 : 0)], ks * 8192); TRD(f.bh[n][ks], bad[((n & 1) ? 1 : 0) + ((n >> 1) ? 2 : 0)], ks * 8192 + 512);
}
template <int... Is> DI void g3_read(bf16x8 (&A)[8][2], FragsB& f, unsigned aad, const unsigned (&bad)[4], std::integer_sequence<int, Is...>) { (g3_read1<Is>(A, f, aad, bad), ...); }
#define PIN3(f) do { asm volatile("s_waitcnt lgkmcnt(0)" : "+v"(A[0][0]), "+v"(A[0][1]), "+v"(A[1][0]), "+v"(A[1][1]), "+v"(A[2][0]), "+v"(A[2][1]), "+v"(A[3][0]), "+v"(A[3][1]), \
        "+v"(A[4][0]), "+v"(A[4][1]), "+v"(A[5][0]), "+v"(A[5][1]), "+v"(A[6][0]), "+v"(A[6][1]), "+v"(A[7][0]), "+v"(A[7][1]) :: "memory"); \
    asm volatile("" : "+v"(f.bl[0][0]), "+v"(f.bl[0][1]), "+v"(f.bl[1][0]), "+v"(f.bl[1][1]), "+v"(f.bl[2][0]), "+v"(f.bl[2][1]), "+v"(f.bl[3][0]), "+v"(f.bl[3][1]), \
        "+v"(f.bh[0][0]), "+v"(f.bh[0][1]), "+v"(f.bh[1][0]), "+v"(f.bh[1][1]), "+v"(f.bh[2][0]), "+v"(f.bh[2][1]), "+v"(f.bh[3][0]), "+v"(f.bh[3][1]) :: "memory"); } while (0)
template <int AO, int BO, int... Is> DI void g3_step(bf16x8 (&A)[8][2], FragsB& cur, FragsB& nxt, unsigned aad, const unsigned (&bad)[4], unsigned ldsA, unsigned ldsB, const unsigned (&voffA)[4], const unsigned (&voffB)[2], const char* sa, const char* sb, std::integer_sequence<int, Is...>) {
    PIN3(cur);
    WAIT_V(12); BAR;
    (g3_row<AO, BO, Is>(A, cur, nxt, aad, bad, ldsA, ldsB, voffA, voffB, sa, sb), ...);
}
template <int I> DI void k3_send1(__amdgpu_buffer_rsrc_t rs, unsigned off, int j) {
    constexpr int m = I >> 1, cg = I & 1;
    if ((m & 3) != j) { const f32x4 a = acc_rd4<4 * (4 * m + 2 * cg)>(), b = acc_rd4<4 * (4 * m + 2 * cg + 1)>();
        __builtin_amdgcn_raw_buffer_store_b128(pack8(a, b), rs, off + (unsigned)(((m & 3) * 16 + (m >> 2) * 2 + cg) * 1024), 0, 16); }
}
template <int... Is> DI void k3_send(__amdgpu_buffer_rsrc_t rs, unsigned off, int j, std::integer_sequence<int, Is...>) { (k3_send1<Is>(rs, off, j), ...); }
template <int J> DI void k3_fin(__amdgpu_buffer_rsrc_t rs, unsigned rd_off, const K3Params& p, int row0, int col0, bool odd, const u32x4 (&xw)[4], const u32x4 (&bw)[2]) {
    u32x4 pv[3][4];
#pragma unroll
    for (int i = 0; i < 4; ++i)
#pragma unroll
        for (int s = 0; s < 3; ++s) pv[s][i] = __builtin_amdgcn_raw_buffer_load_b128(rs, rd_off + (unsigned)((s + (s >= J ? 1 : 0)) * 65536 + i * 1024), 0, 16);
    auto one = [&](auto I_) { constexpr int i = decltype(I_)::value, m = J + 4 * (i >> 1), cg = i & 1;
        f32x4 a = acc_rd4<4 * (4 * m + 2 * cg)>(), b = acc_rd4<4 * (4 * m + 2 * cg + 1)>();
#pragma unroll
        for (int s = 0; s < 3; ++s) { const u32x4 w = pv[s][i];
            a[0] += bf_lo(w[0]); a[1] += bf_hi(w[0]); a[2] += bf_lo(w[1]); a[3] += bf_hi(w[1]); b[0] += bf_lo(w[2]); b[1] += bf_hi(w[2]); b[2] += bf_lo(w[3]); b[3] += bf_hi(w[3]); }
        const f32x4 lo = odd ? b : a, hi = odd ? a : b; const u32x4 x4 = xw[i], b4 = bw[cg];
        f32x4 l2, h2;
        l2[0] = lo[0] + bf_lo(x4[0]) + bf_lo(b4[0]); l2[1] = lo[1] + bf_hi(x4[0]) + bf_hi(b4[0]); l2[2] = lo[2] + bf_lo(x4[1]) + bf_lo(b4[1]); l2[3] = lo[3] + bf_hi(x4[1]) + bf_hi(b4[1]);
        h2[0] = hi[0] + bf_lo(x4[2]) + bf_lo(b4[2]); h2[1] = hi[1] + bf_hi(x4[2]) + bf_hi(b4[2]); h2[2] = hi[2] + bf_lo(x4[3]) + bf_lo(b4[3]); h2[3] = hi[3] + bf_hi(x4[3]) + bf_hi(b4[3]);
        *(u32x4*)(p.out + (size_t)(row0 + 16 * J + 64 * (i >> 1)) * DM + col0 + 32 * cg) = pack8(l2, h2); };
    one(std::integral_constant<int, 0>{}); one(std::integral_constant<int, 1>{}); one(std::integral_constant<int, 2>{}); one(std::integral_constant<int, 3>{});
}

#define STAMP3(i) do {} while (0)
DI void phase3(LAS unsigned char* lds, const K3Params& p, const int wid, int lane, const unsigned tag) {
    const int tid = threadIdx.x, wr = wid >> 1, wc = wid & 1;
    const int xcd = blockIdx.x & 7, s = blockIdx.x >> 3, j = xcd >> 1, pm = 4 * (xcd & 1) + (s >> 3), pn = s & 7, tile = pm * 8 + pn;
    const unsigned l0 = (unsigned)(size_t)lds;
    STAMP3(0);
    asm volatile("" : "+v"(lane));
    unsigned voffA[4], voffB[2];
#pragma unroll
    for (int i = 0; i < 4; ++i) voffA[i] = (unsigned)((wid * 4 + i) * 240 * 1024 + lane * 16);
#pragma unroll
    for (int i = 0; i < 2; ++i) { const int kb = 2 * wid + i, r = lane >> 3, c8 = (lane & 7) ^ ((((r >> 1) & 1) << 2) | (kb & 1)); voffB[i] = (unsigned)((kb * 8 + r) * DM + c8 * 8) * 2u; }
    const char* Abase = (const char*)p.ws + WS_AF + (size_t)pm * 16 * 240 * 1024;
    const char* Bo = (const char*)p.wo + pn * 256; const char* B2 = (const char*)p.w2 + pn * 256 - (size_t)64 * 64 * DM * 2;
    unsigned aad[2], bad[4];
    { const int g = lane >> 4, q = (lane >> 2) & 3, pp = lane & 3;
      const unsigned b0 = l0 + 98304u + (unsigned)(g * 2048 + wc * 1024 + q * 128 + ((pp ^ (((q >> 1) << 2) | (g & 1))) * 16) + (pp & 1) * 8);
      aad[0] = l0 + wr * 16384 + lane * 16; aad[1] = aad[0] + 65536u; bad[0] = b0; bad[1] = b0 ^ 8u; bad[2] = b0 ^ 64u; bad[3] = b0 ^ 72u; }
    const unsigned ldsA0 = l0 + wid * 8192, ldsB0 = l0 + 98304 + wid * 4096;
    bf16x8 A[8][2]; FragsB f0, f1;
#define KK3(t) ((t) < 14 ? 64 + 14 * j + (t) : 16 * j + (t) - 14)
#define PA3(t) (Abase + (size_t)KK3(t) * 2048)
#define PB3(t) (((t) < 14 ? B2 : Bo) + (size_t)KK3(t) * (64 * DM * 2))
#define ST3(sl, FC, FN) do { const int ks = t + 3 < 30 ? t + 3 : 29; \
        g3_step<((((sl) + 1) % 3) == 1 ? 32768 : 0), (((sl) + 1) % 3) * 16384>(A, FC, FN, aad[(((sl) + 1) % 3) == 2], bad, ldsA0 + (sl) * 32768, ldsB0 + (sl) * 16384, voffA, voffB, PA3(ks), PB3(ks), std::make_integer_sequence<int, 8>{}); ++t; } while (0)
#pragma unroll
    for (int i = 0; i < 3; ++i) g3_stage(ldsA0 + i * 32768, ldsB0 + i * 16384, voffA, voffB, PA3(i), PB3(i), std::make_integer_sequence<int, 12>{});
    acc_zero_all(std::make_integer_sequence<int, 32>{});
    WAIT_V(24); BAR;
    g3_read(A, f0, aad[0], bad, std::make_integer_sequence<int, 8>{});
    STAMP3(1);
    const int cidx = lane < 16 ? W_C + (pm * 4 + j) * 64 + lane : -1;
    for (int t = 0; t < 30;) {
        ST3(0, f0, f1); ST3(1, f1, f0);
        if (t == 8 && wid == 0) poll_finish_inv(lds + 148480, p.ws, cidx, tag);
        ST3(2, f0, f1); ST3(0, f1, f0);
        if (t == 4 && wid == 0) poll_issue(lds + 148480, p.ws, cidx);
        ST3(1, f0, f1); ST3(2, f1, f0);
    }
    STAMP3(2);
    asm volatile("s_nop 7\n\ts_nop 7\n\ts_waitcnt lgkmcnt(0)" ::: "memory");
    lane = opaque_lane();
    const rsrc_t rs = mkrs(p.ws + WS_PART + (size_t)tile * 4 * 65536, 4 * 65536);
    const int fr = lane & 15, fq = lane >> 4;
    const int row0 = pm * 256 + wr * 128 + fr, col0 = pn * 128 + wc * 64 + fq * 8;
    u32x4 xw[4], bw[2];
#pragma unroll
    for (int i = 0; i < 4; ++i) xw[i] = *(const u32x4*)(p.x + (size_t)(row0 + 16 * j + 64 * (i >> 1)) * DM + col0 + 32 * (i & 1));
    bw[0] = *(const u32x4*)(p.b2 + col0); bw[1] = *(const u32x4*)(p.b2 + col0 + 32);
    k3_send(rs, (unsigned)(j * 65536 + wid * 4096 + lane * 16), j, std::make_integer_sequence<int, 16>{});
    DRAIN_BAR();
    STAMP3(3);
    if (wid == 0) { if (lane == 0) flag_set(p.ws, W_X + tile * 64 + j, tag); flag_wait(p.ws, lane < 4 ? W_X + tile * 64 + lane : -1, tag); }
    DRAIN_BAR();
    STAMP3(4);
    const unsigned rd_off = (unsigned)(j * 16384 + wid * 4096 + lane * 16);
    if (j == 0) k3_fin<0>(rs, rd_off, p, row0, col0, fq & 1, xw, bw); else if (j == 1) k3_fin<1>(rs, rd_off, p, row0, col0, fq & 1, xw, bw);
    else if (j == 2) k3_fin<2>(rs, rd_off, p, row0, col0, fq & 1, xw, bw); else k3_fin<3>(rs, rd_off, p, row0, col0, fq & 1, xw, bw);
    STAMP3(5);
#undef ST3
}

constexpr int MEGA_LDS = 163840;
typedef const __attribute__((address_space(4))) K1Params* kparg_t;
DI K1Params load_params() {
    kparg_t kp = (kparg_t)__builtin_amdgcn_kernarg_segment_ptr(); asm volatile("" : "+s"(kp));
    K1Params p; p.x = kp->x; p.ckv = kp->ckv; p.wq = kp->wq; p.wuk = kp->wuk; p.wuv = kp->wuv; p.wo = kp->wo; p.w1 = kp->w1; p.b1 = kp->b1; p.w2 = kp->w2; p.b2 = kp->b2; p.out = kp->out; p.ws = kp->ws; return p;
}
__global__ __launch_bounds__(256, 1) void mega(K1Params p_unused) {
    extern __shared__ __attribute__((aligned(16))) unsigned char shm[];
    LAS unsigned char* lds = (LAS unsigned char*)shm;
    const int wid = __builtin_amdgcn_readfirstlane(threadIdx.x >> 6);
    unsigned tag;
    const char* codep;
    { unsigned long long pc; asm volatile("s_getpc_b64 %0" : "=s"(pc)); pc &= ~255ull; const unsigned s_ = blockIdx.x >> 3;
      codep = (const char*)(pc + (s_ < CODEPF ? s_ : 0u) * 4096 + wid * 1024);
      asm volatile("s_mov_b32 m0, %2\n\ts_nop 4\n\tglobal_load_lds_dwordx4 %0, %1" :: "v"((unsigned)(opaque_lane() * 16)), "s"(codep), "s"((unsigned)(size_t)lds + 139264u + wid * 1024u) : "memory", "m0"); }
    const int xcd = blockIdx.x & 7, s = blockIdx.x >> 3;
    int h, qbA, qbB;
    if (s < 16) { h = 4 * xcd + 2 * (s >> 3); qbA = 2 * (s & 7); qbB = 15 - qbA; }
    else { h = 4 * xcd + 2 * ((s - 16) >> 3) + 1; qbB = s & 7; qbA = 15 - qbB; }
    { const K1Params p = load_params(); tag = phase1(lds, p, wid, opaque_lane(), h, qbA > 0 ? 1 : 0, codep); }
    {
        const K1Params p = load_params(); const int lane = opaque_lane();
        if (s >= 30) {
            if (wid == 0) { flag_wait(p.ws, lane < 16 ? W_KV + (h >> 1) * 64 + lane : (lane == 16 ? W_Q + (h >> 1) * 64 + (qbA >> 1) : -1), tag); acquire_all(); }
            DRAIN_BAR();
        } else if (s >= 16) {
            if (wid == 0) flag_wait(p.ws, lane == 0 ? W_Q + (h >> 1) * 64 + (qbA >> 1) : -1, tag);
            DRAIN_BAR();
        }
        unsigned char* ws = p.ws; STAMP2(0);
        attn_pair(lds, ws, h, qbA, qbB, wid, lane, tag, (const char*)p.wo + (size_t)blockIdx.x * 32768 + wid * 1024, (const char*)p.w2 + (size_t)blockIdx.x * 28672 + wid * 1024,
                  lane < 14 ? W_H + (4 * (xcd & 1) + (s >> 3)) * 64 + lane : -1, s < 30, lane == 0 ? W_Q + (h >> 1) * 64 + (qbB >> 1) : -1, codep);
    }
    { const K1Params p = load_params(); phase3(lds, p, wid, opaque_lane(), tag);
      if (blockIdx.x == 0 && wid == 0) {
          const int l2 = opaque_lane();
#pragma unroll
          for (int i = 0; i < 4; ++i) flag_wait(p.ws, W_START + i * 64 + l2, tag);
          if (l2 == 0) flag_set(p.ws, W_E, tag); } }
}

extern "C" void kernel_launch(void* const* d_in, const int* in_sizes, int n_in, void* d_out, int out_size, void* d_ws, size_t ws_size, hipStream_t stream) {
    K1Params p;
    p.x = (const bf16_t*)d_in[0]; p.ckv = (const bf16_t*)d_in[1]; p.wq = (const bf16_t*)d_in[2]; p.wuk = (const bf16_t*)d_in[3]; p.wuv = (const bf16_t*)d_in[4]; p.wo = (const bf16_t*)d_in[5];
    p.w1 = (const bf16_t*)d_in[7]; p.b1 = (const bf16_t*)d_in[8]; p.w2 = (const bf16_t*)d_in[9]; p.b2 = (const bf16_t*)d_in[10]; p.out = (bf16_t*)d_out; p.ws = (unsigned char*)d_ws;
    static bool attr = false;
    if (!attr) { (void)hipFuncSetAttribute((const void*)mega, hipFuncAttributeMaxDynamicSharedMemorySize, MEGA_LDS); attr = true; }
    mega<<<256, 256, MEGA_LDS, stream>>>(p);
}
```
